```python
import torch

def _softmax(x, axis=-1):
    return torch.softmax(x, dim=axis)
def _gelu(x):
    return torch.nn.functional.gelu(x, approximate="tanh")
def _silu(x):
    return torch.nn.functional.silu(x)
def _sigmoid(x):
    return torch.sigmoid(x)
def _layernorm(x, axis=-1, eps=1e-5):
    m = x.mean(dim=axis, keepdim=True)
    v = x.var(dim=axis, keepdim=True, unbiased=False)
    return (x - m) / torch.sqrt(v + eps)
def _scatter_add(tgt, idx, src, axis=0):
    return tgt.clone().index_add_(axis, idx, src)
def _segment_sum(src, seg, n):
    out = torch.zeros((n,) + src.shape[1:], dtype=src.dtype)
    return out.index_add_(0, seg, src)
def _pad(x, pw):
    flat = tuple(v for pair in reversed(pw) for v in pair)
    return torch.nn.functional.pad(x, flat)

kernel_name = 'interleave_layers__sliding_block_cached__glu_gated_glu__decode'

def setup_inputs():

    _w = (lambda a: (lambda t: t.to(torch.bfloat16) if t.is_floating_point() else t)(torch.as_tensor(a)))
    def _A_setup():
        B, Sq, D = 1, 1, 512
        Hq, Hkv, Sk, Dh = 32, 8, 32768, 128
        assert Hq % Hkv == 0, (Hq, Hkv)
        return {
            "x": _w((lambda *s: torch.randn(*s, dtype=torch.bfloat16) * 0.05)(B, Sq, D)),
            "k_cache": _w((lambda *s: torch.randn(*s, dtype=torch.bfloat16) * 1.6)(B, Hkv, Sk, Dh)),
            "v_cache": _w((lambda *s: torch.randn(*s, dtype=torch.bfloat16) * 1.6)(B, Hkv, Sk, Dh)),
            "wq": _w((lambda *s: torch.randn(*s, dtype=torch.bfloat16) * 1.6)(D, Hq * Dh) * (D ** -0.5)),
            "wo": _w((lambda *s: torch.randn(*s, dtype=torch.bfloat16) * 1.6)(Hq * Dh, D) * ((Hq * Dh) ** -0.5)),
        }
    _A_in = _A_setup()

    def _B_setup():
        B, S, D, F = 1, 1, 512, 1792
        return {
            "x": _w((lambda *s: torch.randn(*s, dtype=torch.bfloat16) * 0.05)(B, S, D)),
            "w_gate": _w((lambda *s: torch.randn(*s, dtype=torch.bfloat16) * 0.05)(D, F)),
            "w_up": _w((lambda *s: torch.randn(*s, dtype=torch.bfloat16) * 0.05)(D, F)),
            "w_down": _w((lambda *s: torch.randn(*s, dtype=torch.bfloat16) * 0.05)(F, D)),
        }
    _B_in = _B_setup()

    return {
        "A_x": _A_in["x"],
        "A_k_cache": _A_in["k_cache"],
        "A_v_cache": _A_in["v_cache"],
        "A_wq": _A_in["wq"],
        "A_wo": _A_in["wo"],
        "B_x": _B_in["x"],
        "B_w_gate": _B_in["w_gate"],
        "B_w_up": _B_in["w_up"],
        "B_w_down": _B_in["w_down"],
    }

def reference(A_x, A_k_cache, A_v_cache, A_wq, A_wo, B_x, B_w_gate, B_w_up, B_w_down):
    def _A_ref(x, k_cache, v_cache, wq, wo):
        B, Sq, D = x.shape
        Hq, Dh = 32, 128
        Hkv, Sk = k_cache.shape[1], k_cache.shape[2]
        W = min(4096, max(256, Sk // 4))
        rep = Hq // Hkv
        q = (lambda x, *d: x.permute(*d))((x @ wq).reshape(B, Sq, Hq, Dh), 0, 2, 1, 3)
        k = (lambda x, n, axis: torch.repeat_interleave(x, n, dim=axis))(k_cache[:, :, Sk - W:, :], rep, axis=1)
        v = (lambda x, n, axis: torch.repeat_interleave(x, n, dim=axis))(v_cache[:, :, Sk - W:, :], rep, axis=1)
        scores = torch.einsum("bhqd,bhkd->bhqk", q, k) * (Dh ** -0.5)
        p = _softmax(scores, axis=-1)
        o = torch.einsum("bhqk,bhkd->bhqd", p, v)
        return (lambda x, *d: x.permute(*d))(o, 0, 2, 1, 3).reshape(B, Sq, Hq * Dh) @ wo

    def _B_ref(x, w_gate, w_up, w_down):
        g = torch.einsum("bsd,df->bsf", x, w_gate)
        u = torch.einsum("bsd,df->bsf", x, w_up)
        h = _sigmoid(g) * u
        return torch.einsum("bsf,fd->bsd", h, w_down)

    x = A_x
    x = x + _A_ref(_layernorm(x, axis=-1), A_k_cache, A_v_cache, A_wq, A_wo)
    x = x + _B_ref(_layernorm(x, axis=-1), B_w_gate, B_w_up, B_w_down)
    x = x + _A_ref(_layernorm(x, axis=-1), A_k_cache, A_v_cache, A_wq, A_wo)
    x = x + _B_ref(_layernorm(x, axis=-1), B_w_gate, B_w_up, B_w_down)
    return x
```

```cpp
#include <hip/hip_runtime.h>
#include <hip/hip_bf16.h>
typedef unsigned short u16;
__device__ __forceinline__ float bf2f(u16 b){ return __uint_as_float(((unsigned)b)<<16); }
__device__ __forceinline__ u16 f2bf(float f){ unsigned u=__float_as_uint(f); u += 0x7fffu + ((u>>16)&1u); return (u16)(u>>16); }
__device__ __forceinline__ float rbf(float f){ return bf2f(f2bf(f)); }

constexpr int D=512, HQ=32, HKV=8, SK=32768, DH=128, W=4096, F=1792;

__device__ void ln_block(const float* x, float* xs, float* red){
  int t=threadIdx.x;
  float a=x[t], b=x[t+256];
  float s=a+b;
  for(int o=32;o>=1;o>>=1) s+=__shfl_xor(s,o);
  if((t&63)==0) red[t>>6]=s;
  __syncthreads();
  float m=(red[0]+red[1]+red[2]+red[3])*(1.f/512.f);
  __syncthreads();
  float da=a-m, db=b-m;
  float v=da*da+db*db;
  for(int o=32;o>=1;o>>=1) v+=__shfl_xor(v,o);
  if((t&63)==0) red[t>>6]=v;
  __syncthreads();
  float var=(red[0]+red[1]+red[2]+red[3])*(1.f/512.f);
  float r=rsqrtf(var+1e-5f);
  xs[t]=da*r; xs[t+256]=db*r;
  __syncthreads();
}
__global__ void k_init(const u16* x, float* xf){ int t=threadIdx.x+blockIdx.x*blockDim.x; if(t<D) xf[t]=bf2f(x[t]); }
__global__ void k_q(const float* x, const u16* wq, float* q){
  __shared__ float xs[512]; __shared__ float red[4];
  ln_block(x,xs,red);
  int n=blockIdx.x*256+threadIdx.x;
  float acc=0;
  for(int k=0;k<D;k++) acc+=xs[k]*bf2f(wq[(size_t)k*(HQ*DH)+n]);
  q[n]=acc;
}
__global__ void k_attn(const float* q, const u16* kc, const u16* vc, float* po, float* pml){
  int c=blockIdx.x, h=blockIdx.y, g=h/4, t=threadIdx.x;
  __shared__ float qs[128]; __shared__ float p[128]; __shared__ float red[2];
  qs[t]=q[h*DH+t]; __syncthreads();
  const u16* kr=kc+((size_t)g*SK+(SK-W)+c*128+t)*DH;
  float s=0; for(int d=0;d<DH;d++) s+=qs[d]*bf2f(kr[d]);
  s*=0.08838834764831845f;
  float m=s; for(int o=32;o>=1;o>>=1) m=fmaxf(m,__shfl_xor(m,o));
  if((t&63)==0) red[t>>6]=m; __syncthreads();
  m=fmaxf(red[0],red[1]); __syncthreads();
  float e=__expf(s-m); p[t]=e;
  float l=e; for(int o=32;o>=1;o>>=1) l+=__shfl_xor(l,o);
  if((t&63)==0) red[t>>6]=l; __syncthreads();
  l=red[0]+red[1];
  float o=0; const u16* vr=vc+((size_t)g*SK+(SK-W)+c*128)*DH+t;
  for(int k=0;k<128;k++) o+=p[k]*bf2f(vr[k*DH]);
  po[(h*32+c)*DH+t]=o;
  if(t==0){ pml[(h*32+c)*2]=m; pml[(h*32+c)*2+1]=l; }
}
__global__ void k_comb(const float* po, const float* pml, float* o){
  int h=blockIdx.x, t=threadIdx.x;
  float M=-INFINITY; for(int c=0;c<32;c++) M=fmaxf(M,pml[(h*32+c)*2]);
  float L=0, acc=0; for(int c=0;c<32;c++){ float w=__expf(pml[(h*32+c)*2]-M); L+=w*pml[(h*32+c)*2+1]; acc+=w*po[(h*32+c)*DH+t]; }
  o[h*DH+t]=acc/L;
}
__global__ void k_gemv_res(const float* in, const u16* w, int K, float* x){
  int b=blockIdx.x, t=threadIdx.x;
  float acc[8]={0,0,0,0,0,0,0,0};
  for(int k=t;k<K;k+=256){ float a=in[k]; const u16* r=w+(size_t)k*D+b*8; for(int j=0;j<8;j++) acc[j]+=a*bf2f(r[j]); }
  __shared__ float red[4][8];
  for(int j=0;j<8;j++){ float s=acc[j]; for(int o=32;o>=1;o>>=1) s+=__shfl_xor(s,o); if((t&63)==0) red[t>>6][j]=s; }
  __syncthreads();
  if(t<8) x[b*8+t]+=red[0][t]+red[1][t]+red[2][t]+red[3][t];
}
__global__ void k_gu(const float* x, const u16* wg, const u16* wu, float* h){
  __shared__ float xs[512]; __shared__ float red[4];
  ln_block(x,xs,red);
  int n=blockIdx.x*256+threadIdx.x;
  float g=0,u=0;
  for(int k=0;k<D;k++){ g+=xs[k]*bf2f(wg[(size_t)k*F+n]); u+=xs[k]*bf2f(wu[(size_t)k*F+n]); }
  h[n]=u/(1.f+__expf(-g));
}
__global__ void k_out(const float* x, u16* out){ int t=threadIdx.x+blockIdx.x*blockDim.x; if(t<D) out[t]=f2bf(x[t]); }

extern "C" void kernel_launch(void* const* d_in, const int* in_sizes, int n_in,
                              void* d_out, int out_size, void* d_ws, size_t ws_size,
                              hipStream_t stream) {
  const u16* x=(const u16*)d_in[0]; const u16* kc=(const u16*)d_in[1]; const u16* vc=(const u16*)d_in[2];
  const u16* wq=(const u16*)d_in[3]; const u16* wo=(const u16*)d_in[4];
  const u16* wg=(const u16*)d_in[6]; const u16* wu=(const u16*)d_in[7]; const u16* wd=(const u16*)d_in[8];
  float* ws=(float*)d_ws;
  float* xf=ws; float* q=ws+1024; float* o=q+4096; float* h=o+4096; float* pml=h+2048; float* po=pml+2048;
  k_init<<<2,256,0,stream>>>(x,xf);
  for(int l=0;l<2;l++){
    k_q<<<16,256,0,stream>>>(xf,wq,q);
    k_attn<<<dim3(32,32),128,0,stream>>>(q,kc,vc,po,pml);
    k_comb<<<32,128,0,stream>>>(po,pml,o);
    k_gemv_res<<<64,256,0,stream>>>(o,wo,4096,xf);
    k_gu<<<7,256,0,stream>>>(xf,wg,wu,h);
    k_gemv_res<<<64,256,0,stream>>>(h,wd,F,xf);
  }
  k_out<<<2,256,0,stream>>>(xf,(u16*)d_out);
}
```

```cpp
#include <hip/hip_runtime.h>
#include <hip/hip_bf16.h>
typedef unsigned short u16;
__device__ __forceinline__ float bf2f(u16 b){ return __uint_as_float(((unsigned)b)<<16); }
__device__ __forceinline__ u16 f2bf(float f){ unsigned u=__float_as_uint(f); u += 0x7fffu + ((u>>16)&1u); return (u16)(u>>16); }
__device__ __forceinline__ float rbf(float f){ return bf2f(f2bf(f)); }

constexpr int D=512, HQ=32, HKV=8, SK=32768, DH=128, W=4096, F=1792;

__device__ void ln_block(const float* x, float* xs, float* red){
  int t=threadIdx.x;
  float a=x[t], b=x[t+256];
  float s=a+b;
  for(int o=32;o>=1;o>>=1) s+=__shfl_xor(s,o);
  if((t&63)==0) red[t>>6]=s;
  __syncthreads();
  float m=(red[0]+red[1]+red[2]+red[3])*(1.f/512.f);
  __syncthreads();
  float da=a-m, db=b-m;
  float v=da*da+db*db;
  for(int o=32;o>=1;o>>=1) v+=__shfl_xor(v,o);
  if((t&63)==0) red[t>>6]=v;
  __syncthreads();
  float var=rbf((red[0]+red[1]+red[2]+red[3])*(1.f/512.f));
  float sd=rbf(sqrtf(rbf(var+1e-5f)));
  float mb=rbf(m);
  xs[t]=rbf(rbf(a-mb)/sd); xs[t+256]=rbf(rbf(b-mb)/sd);
  __syncthreads();
}
__global__ void k_init(const u16* x, float* xf){ int t=threadIdx.x+blockIdx.x*blockDim.x; if(t<D) xf[t]=bf2f(x[t]); }
__global__ void k_q(const float* x, const u16* wq, float* q){
  __shared__ float xs[512]; __shared__ float red[4];
  ln_block(x,xs,red);
  int n=blockIdx.x*256+threadIdx.x;
  float acc=0;
  for(int k=0;k<D;k++) acc+=xs[k]*bf2f(wq[(size_t)k*(HQ*DH)+n]);
  q[n]=rbf(acc);
}
__global__ void k_attn(const float* q, const u16* kc, const u16* vc, float* po, float* pml){
  int c=blockIdx.x, h=blockIdx.y, g=h/4, t=threadIdx.x;
  __shared__ float qs[128]; __shared__ float p[128]; __shared__ float red[2];
  qs[t]=q[h*DH+t]; __syncthreads();
  const u16* kr=kc+((size_t)g*SK+(SK-W)+c*128+t)*DH;
  float s=0; for(int d=0;d<DH;d++) s+=qs[d]*bf2f(kr[d]);
  s=rbf(rbf(s)*0.08838834764831845f);
  float m=s; for(int o=32;o>=1;o>>=1) m=fmaxf(m,__shfl_xor(m,o));
  if((t&63)==0) red[t>>6]=m; __syncthreads();
  m=fmaxf(red[0],red[1]); __syncthreads();
  float e=__expf(s-m); p[t]=e;
  float l=e; for(int o=32;o>=1;o>>=1) l+=__shfl_xor(l,o);
  if((t&63)==0) red[t>>6]=l; __syncthreads();
  l=red[0]+red[1];
  float o=0; const u16* vr=vc+((size_t)g*SK+(SK-W)+c*128)*DH+t;
  for(int k=0;k<128;k++) o+=p[k]*bf2f(vr[k*DH]);
  po[(h*32+c)*DH+t]=o;
  if(t==0){ pml[(h*32+c)*2]=m; pml[(h*32+c)*2+1]=l; }
}
__global__ void k_comb(const float* po, const float* pml, float* o){
  int h=blockIdx.x, t=threadIdx.x;
  float M=-INFINITY; for(int c=0;c<32;c++) M=fmaxf(M,pml[(h*32+c)*2]);
  float L=0, acc=0; for(int c=0;c<32;c++){ float w=__expf(pml[(h*32+c)*2]-M); L+=w*pml[(h*32+c)*2+1]; acc+=w*po[(h*32+c)*DH+t]; }
  o[h*DH+t]=rbf(acc/L);
}
__global__ void k_gemv_res(const float* in, const u16* w, int K, float* x){
  int b=blockIdx.x, t=threadIdx.x;
  float acc[8]={0,0,0,0,0,0,0,0};
  for(int k=t;k<K;k+=256){ float a=in[k]; const u16* r=w+(size_t)k*D+b*8; for(int j=0;j<8;j++) acc[j]+=a*bf2f(r[j]); }
  __shared__ float red[4][8];
  for(int j=0;j<8;j++){ float s=acc[j]; for(int o=32;o>=1;o>>=1) s+=__shfl_xor(s,o); if((t&63)==0) red[t>>6][j]=s; }
  __syncthreads();
  if(t<8) x[b*8+t]=rbf(x[b*8+t]+rbf(red[0][t]+red[1][t]+red[2][t]+red[3][t]));
}
__global__ void k_gu(const float* x, const u16* wg, const u16* wu, float* h){
  __shared__ float xs[512]; __shared__ float red[4];
  ln_block(x,xs,red);
  int n=blockIdx.x*256+threadIdx.x;
  float g=0,u=0;
  for(int k=0;k<D;k++){ g+=xs[k]*bf2f(wg[(size_t)k*F+n]); u+=xs[k]*bf2f(wu[(size_t)k*F+n]); }
  g=rbf(g); u=rbf(u); h[n]=rbf(rbf(1.f/(1.f+__expf(-g)))*u);
}
__global__ void k_out(const float* x, u16* out){ int t=threadIdx.x+blockIdx.x*blockDim.x; if(t<D) out[t]=f2bf(x[t]); }

extern "C" void kernel_launch(void* const* d_in, const int* in_sizes, int n_in,
                              void* d_out, int out_size, void* d_ws, size_t ws_size,
                              hipStream_t stream) {
  const u16* x=(const u16*)d_in[0]; const u16* kc=(const u16*)d_in[1]; const u16* vc=(const u16*)d_in[2];
  const u16* wq=(const u16*)d_in[3]; const u16* wo=(const u16*)d_in[4];
  const u16* wg=(const u16*)d_in[6]; const u16* wu=(const u16*)d_in[7]; const u16* wd=(const u16*)d_in[8];
  float* ws=(float*)d_ws;
  float* xf=ws; float* q=ws+1024; float* o=q+4096; float* h=o+4096; float* pml=h+2048; float* po=pml+2048;
  k_init<<<2,256,0,stream>>>(x,xf);
  for(int l=0;l<2;l++){
    k_q<<<16,256,0,stream>>>(xf,wq,q);
    k_attn<<<dim3(32,32),128,0,stream>>>(q,kc,vc,po,pml);
    k_comb<<<32,128,0,stream>>>(po,pml,o);
    k_gemv_res<<<64,256,0,stream>>>(o,wo,4096,xf);
    k_gu<<<7,256,0,stream>>>(xf,wg,wu,h);
    k_gemv_res<<<64,256,0,stream>>>(h,wd,F,xf);
  }
  k_out<<<2,256,0,stream>>>(xf,(u16*)d_out);
}
```

```cpp
#include <hip/hip_runtime.h>

typedef unsigned short u16;
typedef unsigned u32;
typedef unsigned long long u64;
typedef u32 v4u __attribute__((ext_vector_type(4)));
typedef u32 v2u __attribute__((ext_vector_type(2)));
typedef float v4f __attribute__((ext_vector_type(4)));
typedef float v2f __attribute__((ext_vector_type(2)));
typedef __bf16 bf16x8 __attribute__((ext_vector_type(8)));
typedef __bf16 bf16x2 __attribute__((ext_vector_type(2)));
#define GAS __attribute__((address_space(1)))
#define LAS __attribute__((address_space(3)))

constexpr int SK = 32768, WIN = 4096, FF = 1792;
constexpr u32 SENT = 0xFFFFFFFFu;
constexpr int AUX_SC1 = 16;
constexpr u32 SPIN_CAP = 200000;

constexpr int WS_EPOCH = 0;
constexpr int WS_RDY = 4096;
constexpr int WS_H1 = 8192;
constexpr int WS_H2 = 65536;
constexpr int H2_SLOT = 1152, H2_L = 256 * H2_SLOT;
constexpr int WS_H3 = WS_H2 + 2 * H2_L;
constexpr int WS_H4 = WS_H3 + 2 * 16384;
constexpr int WS_H5 = WS_H4 + 2 * 32768;
constexpr int WS_END = WS_H5 + 2 * 16384;
constexpr int WS_STAMP = 1 << 20;
constexpr int WS_BYTES = 2 << 20;

constexpr int L_QS = 0;
constexpr int L_PO = 1024;
constexpr int L_PM = 9216;
constexpr int L_ACT = 9472;
constexpr int L_ST = 10496;
constexpr int L_RED = 10560;
constexpr int LDS_BYTES = 96 * 1024;

#define D_H1 0
#define D_H2 4

struct Params {
  const u16* x; const u16* kc; const u16* vc; const u16* wq; const u16* wo;
  const u16* wg; const u16* wu; const u16* wd; u16* out; unsigned char* ws;
};

__device__ __forceinline__ float rbf(float x) { return (float)(__bf16)x; }
__device__ __forceinline__ u32 pk2(float lo, float hi) { bf16x2 v = {(__bf16)lo, (__bf16)hi}; return __builtin_bit_cast(u32, v); }
__device__ __forceinline__ float bflo(u32 w) { return __uint_as_float(w << 16); }
__device__ __forceinline__ float bfhi(u32 w) { return __uint_as_float(w & 0xffff0000u); }
__device__ __forceinline__ u32 nosent(u32 v) { return v == SENT ? 0x7FC07FC0u : v; }
__device__ __forceinline__ v4u ldnt(const void* p) { return __builtin_nontemporal_load((const GAS v4u*)p); }
__device__ __forceinline__ v4f mfma(v4u a, v4u b, v4f c) {
  return __builtin_amdgcn_mfma_f32_16x16x32_bf16(__builtin_bit_cast(bf16x8, a), __builtin_bit_cast(bf16x8, b), c, 0, 0, 0);
}
template <int CTRL> __device__ __forceinline__ float dpp(float x) {
  return __builtin_bit_cast(float, __builtin_amdgcn_mov_dpp(__builtin_bit_cast(int, x), CTRL, 0xf, 0xf, true));
}
constexpr int XOR1 = 0xB1, XOR2 = 0x4E, XOR7 = 0x141, XOR8 = 0x128, ROR4 = 0x124, XOR15 = 0x140;
__device__ __forceinline__ float x16_max(float x) {
  auto s = __builtin_amdgcn_permlane16_swap(__float_as_uint(x), __float_as_uint(x), false, false);
  x = fmaxf(__uint_as_float(s[0]), __uint_as_float(s[1]));
  auto t = __builtin_amdgcn_permlane32_swap(__float_as_uint(x), __float_as_uint(x), false, false);
  return fmaxf(__uint_as_float(t[0]), __uint_as_float(t[1]));
}
__device__ __forceinline__ float x16_sum(float x) {
  auto s = __builtin_amdgcn_permlane16_swap(__float_as_uint(x), __float_as_uint(x), false, false);
  x = __uint_as_float(s[0]) + __uint_as_float(s[1]);
  auto t = __builtin_amdgcn_permlane32_swap(__float_as_uint(x), __float_as_uint(x), false, false);
  return __uint_as_float(t[0]) + __uint_as_float(t[1]);
}
__device__ __forceinline__ float x32_sum(float x) {
  auto t = __builtin_amdgcn_permlane32_swap(__float_as_uint(x), __float_as_uint(x), false, false);
  return __uint_as_float(t[0]) + __uint_as_float(t[1]);
}
__device__ __forceinline__ float x16only_sum(float x) {
  auto s = __builtin_amdgcn_permlane16_swap(__float_as_uint(x), __float_as_uint(x), false, false);
  return __uint_as_float(s[0]) + __uint_as_float(s[1]);
}
__device__ __forceinline__ float row_sum(float x) {
  x += dpp<XOR1>(x); x += dpp<XOR2>(x); x += dpp<XOR7>(x); x += dpp<XOR15>(x);
  return x;
}
__device__ __forceinline__ void bar() { __syncthreads(); }
#define LGKM0() asm volatile("s_waitcnt lgkmcnt(0)" ::: "memory")
#define SB() __builtin_amdgcn_sched_barrier(0)

#define STAMP(id) do { } while (0)

__device__ __forceinline__ void tr8(const v4u (&r)[8], v4u (&f)[8]) {
#pragma unroll
  for (int c = 0; c < 8; ++c) {
    const u32 sel = (c & 1) ? 0x07060302u : 0x05040100u;
#pragma unroll
    for (int m = 0; m < 4; ++m) f[c][m] = __builtin_amdgcn_perm(r[2 * m + 1][c >> 1], r[2 * m][c >> 1], sel);
  }
}
__device__ __forceinline__ bool nos4(v4u v) { return (v.x != SENT) & (v.y != SENT) & (v.z != SENT) & (v.w != SENT); }

__global__ void __launch_bounds__(512, 2) fused_decode(Params p) {
  __shared__ __attribute__((aligned(16))) unsigned char lds_raw[LDS_BYTES];
  LAS unsigned char* L = (LAS unsigned char*)lds_raw;
  const int tid = threadIdx.x, lane = tid & 63, wave = __builtin_amdgcn_readfirstlane(tid >> 6);
  const int cu = blockIdx.x, g = cu & 7, j = cu >> 3, role = j >> 3, ri = j & 7;
  const int q4 = lane >> 4, n16 = lane & 15;
  const __amdgpu_buffer_rsrc_t rs = __builtin_amdgcn_make_buffer_rsrc(p.ws, 0, WS_BYTES, 0x00020000);
  const int nblk = g < 4 ? 4 : 3;
  constexpr float LOG2E = 1.4426950408889634f;

  if (wave < 4) {
    STAMP(wave == 0 ? 0 : 63);
    const size_t kb = ((size_t)g * SK + (SK - WIN) + j * 128 + wave * 32) * 128;
    v4u kf[2][4], vr[8], vf[8];
#pragma unroll
    for (int t = 0; t < 2; ++t)
#pragma unroll
      for (int s = 0; s < 4; ++s) kf[t][s] = ldnt(p.kc + kb + (t * 16 + n16) * 128 + s * 32 + q4 * 8);
#pragma unroll
    for (int e = 0; e < 8; ++e) vr[e] = ldnt(p.vc + kb + (16 * (e >> 2) + 4 * q4 + (e & 3)) * 128 + n16 * 8);
    SB();
#pragma unroll
    for (int l = 0; l < 2; ++l) {
      if (role == 0) { bar(); bar(); }
      bar();
      if (wave == 0) STAMP(l * 16 + 3);
      v4u qf[4];
#pragma unroll
      for (int s = 0; s < 4; ++s) qf[s] = *(const LAS v4u*)(L + L_QS + ((n16 & 3) * 128 + s * 32 + q4 * 8) * 2);
      v4f sc[2];
#pragma unroll
      for (int t = 0; t < 2; ++t) {
        sc[t] = v4f{0.f, 0.f, 0.f, 0.f};
#pragma unroll
        for (int s = 0; s < 4; ++s) sc[t] = mfma(kf[t][s], qf[s], sc[t]);
      }
      float s8[8];
#pragma unroll
      for (int e = 0; e < 8; ++e) s8[e] = rbf(rbf(sc[e >> 2][e & 3]) * 0.08838834764831845f) * LOG2E;
      float m = fmaxf(fmaxf(fmaxf(s8[0], s8[1]), fmaxf(s8[2], s8[3])), fmaxf(fmaxf(s8[4], s8[5]), fmaxf(s8[6], s8[7])));
      m = x16_max(m);
      float lsum = 0.f;
#pragma unroll
      for (int e = 0; e < 8; ++e) { s8[e] = __builtin_amdgcn_exp2f(s8[e] - m); lsum += s8[e]; }
      lsum = x16_sum(lsum);
      v4u pa = {pk2(s8[0], s8[1]), pk2(s8[2], s8[3]), pk2(s8[4], s8[5]), pk2(s8[6], s8[7])};
      if (wave == 0) STAMP(l * 16 + 4);
      if (l == 0) tr8(vr, vf);
      v4f o[8];
#pragma unroll
      for (int c = 0; c < 8; ++c) o[c] = mfma(pa, vf[c], v4f{0.f, 0.f, 0.f, 0.f});
      {
        const v4f a = q4 == 0 ? o[0] : q4 == 1 ? o[2] : q4 == 2 ? o[4] : o[6];
        const v4f b = q4 == 0 ? o[1] : q4 == 1 ? o[3] : q4 == 2 ? o[5] : o[7];
#pragma unroll
        for (int r = 0; r < 4; ++r) *(LAS v2f*)(L + L_PO + ((wave * 4 + r) * 128 + n16 * 8 + q4 * 2) * 4) = v2f{a[r], b[r]};
        if (lane < 4) *(LAS v2f*)(L + L_PM + (lane * 4 + wave) * 8) = v2f{m, lsum};
      }
      bar();
      if (wave == 0) STAMP(l * 16 + 5);
      {
        const v4f ml0 = *(const LAS v4f*)(L + L_PM + wave * 32), ml1 = *(const LAS v4f*)(L + L_PM + wave * 32 + 16);
        v2f pv[4];
#pragma unroll
        for (int w = 0; w < 4; ++w) pv[w] = *(const LAS v2f*)(L + L_PO + ((w * 4 + wave) * 128 + lane * 2) * 4);
        const float M = fmaxf(fmaxf(ml0[0], ml0[2]), fmaxf(ml1[0], ml1[2]));
        const float s0 = __builtin_amdgcn_exp2f(ml0[0] - M), s1 = __builtin_amdgcn_exp2f(ml0[2] - M);
        const float s2 = __builtin_amdgcn_exp2f(ml1[0] - M), s3 = __builtin_amdgcn_exp2f(ml1[2] - M);
        const float Ls = (s0 * ml0[1] + s1 * ml0[3]) + (s2 * ml1[1] + s3 * ml1[3]);
        const float inv = __builtin_amdgcn_rcpf(Ls);
        const float o0 = ((s0 * pv[0][0] + s1 * pv[1][0]) + (s2 * pv[2][0] + s3 * pv[3][0])) * inv;
        const float o1 = ((s0 * pv[0][1] + s1 * pv[1][1]) + (s2 * pv[2][1] + s3 * pv[3][1])) * inv;
        const int slot = WS_H2 + l * H2_L + (g * 32 + j) * H2_SLOT;
        __builtin_amdgcn_raw_buffer_store_b32(nosent(pk2(o0, o1)), rs, slot + wave * 256 + lane * 4, 0, AUX_SC1);
        if (lane == 0) __builtin_amdgcn_raw_buffer_store_b32(nosent(__float_as_uint(M + __builtin_amdgcn_logf(Ls))), rs, slot + 1024 + wave * 4, 0, AUX_SC1);
      }
      if (wave == 0) STAMP(l * 16 + 6);
      if (role == 1 || role == 3) bar();
      if (role == 2) { bar(); bar(); }
    }
    return;
  }

  const int w = wave - 4;
  const int kr = n16 >> 3, cb = n16 & 7;
  if (w == 3) {
    const v4u sv = {SENT, SENT, SENT, SENT};
#pragma unroll
    for (int l = 0; l < 2; ++l) {
      const int slot = WS_H2 + l * H2_L + (g * 32 + j) * H2_SLOT;
      __builtin_amdgcn_raw_buffer_store_b128(sv, rs, slot + lane * 16, 0, AUX_SC1);
      int off = slot + 1024;
      bool on = lane == 63;
      if (role == 1) { if (lane < 16) { off = WS_H3 + l * 16384 + g * 2048 + ri * 256 + lane * 16; on = true; } }
      if (role == 2) { if (lane < 32) { off = WS_H4 + l * 32768 + g * 4096 + ri * 512 + lane * 16; on = true; } }
      if (role == 3) { if (lane < 16) { off = WS_H5 + l * 16384 + g * 2048 + ri * 256 + lane * 16; on = true; } }
      if (on) __builtin_amdgcn_raw_buffer_store_b128(sv, rs, off, 0, AUX_SC1);
    }
  }
  const u32 ep_raw = __builtin_amdgcn_raw_buffer_load_b32(rs, WS_EPOCH, 0, AUX_SC1);

  const u16* wsrc[2]; int pitch; bool wact = true;
  {
    const int rl = 32 * kr + 8 * q4;
    if (role == 0) { pitch = 4096; wsrc[0] = p.wq + (size_t)(128 * w + rl) * 4096 + g * 512 + ri * 64 + cb * 8; wsrc[1] = wsrc[0] + 64 * 4096; }
    else if (role == 1) { pitch = 512; wsrc[0] = p.wo + (size_t)(g * 512 + 128 * w + rl) * 512 + ri * 64 + cb * 8; wsrc[1] = wsrc[0] + 64 * 512; }
    else if (role == 2) {
      const int bi = ri >> 1, kh = ri & 1; wact = bi < nblk; const int blk = wact ? g + 8 * bi : g;
      pitch = FF; const size_t o = (size_t)(256 * kh + 64 * w + rl) * FF + blk * 64 + cb * 8; wsrc[0] = p.wg + o; wsrc[1] = p.wu + o;
    } else {
      wact = w < nblk; const int blk = wact ? g + 8 * w : g;
      pitch = 512; wsrc[0] = p.wd + (size_t)(blk * 64 + rl) * 512 + ri * 64 + cb * 8; wsrc[1] = wsrc[0];
    }
  }
  v4u wr[2][8], wf[2][8];
  auto issue_w = [&]() {
#pragma unroll
    for (int e = 0; e < 8; ++e) wr[0][e] = ldnt(wsrc[0] + (size_t)e * pitch);
    if (role != 3) {
#pragma unroll
      for (int e = 0; e < 8; ++e) wr[1][e] = ldnt(wsrc[1] + (size_t)e * pitch);
    } else {
#pragma unroll
      for (int e = 0; e < 8; ++e) wr[1][e] = v4u{0u, 0u, 0u, 0u};
    }
    SB();
  };
  const int l31 = lane & 31, hf = lane >> 5;
  int xcol;
  if (role == 2) { const int kh = ri & 1; xcol = (l31 < 16) ? 256 * kh + 64 * w + 4 * l31 : 256 * (1 - kh) + 64 * w + 4 * (l31 - 16); }
  else xcol = 128 * w + 4 * l31;
  const v2u xv = *(const GAS v2u*)(p.x + xcol);
  SB();
  if (role == 0 || w != 3) issue_w();
  SB();
  float xr[4];
  xr[0] = bflo(xv.x); xr[1] = bfhi(xv.x); xr[2] = bflo(xv.y); xr[3] = bfhi(xv.y);
  const u32 tag = __builtin_amdgcn_readfirstlane(ep_raw) + 1u;
  if (w == 3) {
    asm volatile("s_waitcnt vmcnt(0)" ::: "memory");
    if (lane == 0) __builtin_amdgcn_raw_buffer_store_b64(v2u{tag, tag}, rs, WS_RDY + cu * 8, 0, AUX_SC1);
  }

  auto gather_x = [&](int base) {
    v4u v[4];
    for (u32 spins = 0; spins < SPIN_CAP; ++spins) {
#pragma unroll
      for (int k = 0; k < 4; ++k) v[k] = __builtin_amdgcn_raw_buffer_load_b128(rs, base + (hf * 4 + k) * 2048 + xcol * 4, 0, AUX_SC1);
      const bool ok = nos4(v[0]) & nos4(v[1]) & nos4(v[2]) & nos4(v[3]);
      if (__all(ok)) break;
      __builtin_amdgcn_s_sleep(2);
    }
#pragma unroll
    for (int c = 0; c < 4; ++c) {
      float s = (__uint_as_float(v[0][c]) + __uint_as_float(v[1][c])) + (__uint_as_float(v[2][c]) + __uint_as_float(v[3][c]));
      s = x32_sum(s);
      xr[c] = rbf(xr[c] + rbf(s));
    }
  };
  auto layernorm = [&]() {
    float s = (xr[0] + xr[1]) + (xr[2] + xr[3]);
    s = x16only_sum(row_sum(s));
    const float mw = s * (1.f / 128.f);
    float d0 = xr[0] - mw, d1 = xr[1] - mw, d2 = xr[2] - mw, d3 = xr[3] - mw;
    float m2 = (d0 * d0 + d1 * d1) + (d2 * d2 + d3 * d3);
    m2 = x16only_sum(row_sum(m2));
    if (lane == 0) *(LAS v2f*)(L + L_ST + w * 8) = v2f{mw, m2};
    bar();
    const v4f a = *(const LAS v4f*)(L + L_ST), b = *(const LAS v4f*)(L + L_ST + 16);
    const float mean = ((a[0] + a[2]) + (b[0] + b[2])) * 0.25f;
    const float e0 = a[0] - mean, e1 = a[2] - mean, e2 = b[0] - mean, e3 = b[2] - mean;
    const float M2 = ((a[1] + a[3]) + (b[1] + b[3])) + 128.f * ((e0 * e0 + e1 * e1) + (e2 * e2 + e3 * e3));
    const float var = rbf(M2 * (1.f / 512.f));
    const float sd = rbf(__builtin_sqrtf(rbf(var + 1e-5f)));
    const float mb = rbf(mean);
    const float inv = 1.f / sd;
    v2u o;
    o.x = pk2(rbf(xr[0] - mb) * inv, rbf(xr[1] - mb) * inv);
    o.y = pk2(rbf(xr[2] - mb) * inv, rbf(xr[3] - mb) * inv);
    if (hf == 0) *(LAS v2u*)(L + L_ACT + xcol * 2) = o;
    LGKM0();
  };
  auto put_red = [&](const v4f (&acc)[8], int slot) {
    float v[8];
#pragma unroll
    for (int c = 0; c < 8; ++c) { const float t = kr ? acc[c][1] : acc[c][0]; v[c] = t + dpp<XOR8>(t); }
    if (lane < 8) {
      *(LAS v4f*)(L + L_RED + (slot * 64 + cb * 8) * 4) = v4f{v[0], v[1], v[2], v[3]};
      *(LAS v4f*)(L + L_RED + (slot * 64 + cb * 8 + 4) * 4) = v4f{v[4], v[5], v[6], v[7]};
    }
  };
  const v4f z4 = {0.f, 0.f, 0.f, 0.f};

#pragma unroll
  for (int l = 0; l < 2; ++l) {
    if (role == 0) {
      if (l == 0) { tr8(wr[0], wf[0]); tr8(wr[1], wf[1]); }
      else gather_x(WS_H5);
      if (w == 0) STAMP(l * 16 + 1);
      layernorm();
      v4f acc[8];
      const v4u a0 = *(const LAS v4u*)(L + L_ACT + (128 * w + 32 * (n16 & 1) + 8 * q4) * 2);
      const v4u a1 = *(const LAS v4u*)(L + L_ACT + (128 * w + 64 + 32 * (n16 & 1) + 8 * q4) * 2);
#pragma unroll
      for (int c = 0; c < 8; ++c) acc[c] = mfma(a1, wf[1][c], mfma(a0, wf[0][c], z4));
      put_red(acc, w);
      bar();
      if (w == 0) {
        const LAS float* rd = (const LAS float*)(L + L_RED);
        const float qv = (rd[lane] + rd[64 + lane]) + (rd[128 + lane] + rd[192 + lane]);
        const float qn = dpp<XOR1>(qv);
        if ((lane & 1) == 0) __builtin_amdgcn_raw_buffer_store_b64(v2u{pk2(qv, qn), tag}, rs, WS_H1 + l * 16384 + g * 2048 + ri * 256 + lane * 4, 0, AUX_SC1);
        STAMP(l * 16 + 2);
      }
    }
    if (w == 3) {
      if (D_H1 && (l == 1 || role == 0)) __builtin_amdgcn_s_sleep(D_H1);
      v4u v[2];
      for (u32 spins = 0; spins < SPIN_CAP; ++spins) {
#pragma unroll
        for (int k = 0; k < 2; ++k) v[k] = __builtin_amdgcn_raw_buffer_load_b128(rs, WS_H1 + l * 16384 + g * 2048 + k * 1024 + lane * 16, 0, AUX_SC1);
        const bool ok = (v[0].y == tag) & (v[0].w == tag) & (v[1].y == tag) & (v[1].w == tag);
        if (__all(ok)) break;
        __builtin_amdgcn_s_sleep(2);
      }
#pragma unroll
      for (int k = 0; k < 2; ++k) *(LAS v2u*)(L + L_QS + k * 512 + lane * 8) = v2u{v[k].x, v[k].z};
    }
    bar();
    if (l == 0) {
      if (w == 3) {
        v4u v[2];
        for (u32 spins = 0; spins < SPIN_CAP; ++spins) {
#pragma unroll
          for (int k = 0; k < 2; ++k) v[k] = __builtin_amdgcn_raw_buffer_load_b128(rs, WS_RDY + k * 1024 + lane * 16, 0, AUX_SC1);
          const bool ok = (v[0].x == tag) & (v[0].z == tag) & (v[1].x == tag) & (v[1].z == tag);
          if (__all(ok)) break;
          __builtin_amdgcn_s_sleep(2);
        }
        if (role != 0) issue_w();
      }
    }
    bar();
    if (l == 0 && role != 0) { tr8(wr[0], wf[0]); if (role != 3) tr8(wr[1], wf[1]); }
    if (role == 1) {
      if (D_H2) __builtin_amdgcn_s_sleep(D_H2);
      v4u pr[8]; float lse[8];
      const int hb = WS_H2 + l * H2_L + (g * 32 + 8 * q4) * H2_SLOT;
      for (u32 spins = 0; spins < SPIN_CAP; ++spins) {
        bool ok = true;
#pragma unroll
        for (int e = 0; e < 8; ++e) lse[e] = __uint_as_float(__builtin_amdgcn_raw_buffer_load_b32(rs, hb + e * H2_SLOT + 1024 + w * 4, 0, AUX_SC1));
#pragma unroll
        for (int e = 0; e < 8; ++e) pr[e] = __builtin_amdgcn_raw_buffer_load_b128(rs, hb + e * H2_SLOT + w * 256 + n16 * 16, 0, AUX_SC1);
#pragma unroll
        for (int e = 0; e < 8; ++e) ok &= nos4(pr[e]) & (__float_as_uint(lse[e]) != SENT);
        if (__all(ok)) break;
        __builtin_amdgcn_s_sleep(2);
      }
      if (w == 0) STAMP(l * 16 + 7);
      float M = fmaxf(fmaxf(fmaxf(lse[0], lse[1]), fmaxf(lse[2], lse[3])), fmaxf(fmaxf(lse[4], lse[5]), fmaxf(lse[6], lse[7])));
      M = x16_max(M);
      float Ls = 0.f;
#pragma unroll
      for (int e = 0; e < 8; ++e) { lse[e] = __builtin_amdgcn_exp2f(lse[e] - M); Ls += lse[e]; }
      Ls = x16_sum(Ls);
      const float inv = __builtin_amdgcn_rcpf(Ls);
#pragma unroll
      for (int e = 0; e < 8; ++e) { const float wv = lse[e] * inv, hi = rbf(wv); lse[e] = (n16 & 1) ? wv - hi : hi; }
      const v4u wa = {pk2(lse[0], lse[1]), pk2(lse[2], lse[3]), pk2(lse[4], lse[5]), pk2(lse[6], lse[7])};
      v4u pf[8];
      tr8(pr, pf);
      float ov[8];
#pragma unroll
      for (int c = 0; c < 8; ++c) { const v4f d = mfma(wa, pf[c], z4); ov[c] = d[0] + d[1]; }
      if (q4 == 0) *(LAS v4u*)(L + L_ACT + (128 * w + 8 * n16) * 2) = v4u{pk2(ov[0], ov[1]), pk2(ov[2], ov[3]), pk2(ov[4], ov[5]), pk2(ov[6], ov[7])};
      LGKM0();
      v4f acc[8];
      const v4u a0 = *(const LAS v4u*)(L + L_ACT + (128 * w + 32 * (n16 & 1) + 8 * q4) * 2);
      const v4u a1 = *(const LAS v4u*)(L + L_ACT + (128 * w + 64 + 32 * (n16 & 1) + 8 * q4) * 2);
#pragma unroll
      for (int c = 0; c < 8; ++c) acc[c] = mfma(a1, wf[1][c], mfma(a0, wf[0][c], z4));
      put_red(acc, w);
      bar();
      if (w == 0) {
        const LAS float* rd = (const LAS float*)(L + L_RED);
        const float y = (rd[lane] + rd[64 + lane]) + (rd[128 + lane] + rd[192 + lane]);
        __builtin_amdgcn_raw_buffer_store_b32(nosent(__float_as_uint(y)), rs, WS_H3 + l * 16384 + g * 2048 + ri * 256 + lane * 4, 0, AUX_SC1);
        STAMP(l * 16 + 8);
      }
    } else if (role == 2) {
      if (l == 1) gather_x(WS_H5);
      gather_x(WS_H3 + l * 16384);
      if (w == 0) STAMP(l * 16 + 9);
      layernorm();
      const int kh = ri & 1;
      const v4u a0 = *(const LAS v4u*)(L + L_ACT + (256 * kh + 64 * w + 32 * (n16 & 1) + 8 * q4) * 2);
      v4f acc[8];
#pragma unroll
      for (int c = 0; c < 8; ++c) acc[c] = mfma(a0, wf[0][c], z4);
      put_red(acc, w);
#pragma unroll
      for (int c = 0; c < 8; ++c) acc[c] = mfma(a0, wf[1][c], z4);
      put_red(acc, 4 + w);
      bar();
      if (w < 2 && wact) {
        const LAS float* rd = (const LAS float*)(L + L_RED) + w * 256;
        const float y = (rd[lane] + rd[64 + lane]) + (rd[128 + lane] + rd[192 + lane]);
        __builtin_amdgcn_raw_buffer_store_b32(nosent(__float_as_uint(y)), rs, WS_H4 + l * 32768 + g * 4096 + ri * 512 + w * 256 + lane * 4, 0, AUX_SC1);
      }
      if (w == 0) STAMP(l * 16 + 10);
    } else if (role == 3) {
      v4f acc[8];
      if (wact) {
        u32 v[4];
        const int hb = WS_H4 + l * 32768 + g * 4096 + w * 1024 + lane * 4;
        for (u32 spins = 0; spins < SPIN_CAP; ++spins) {
#pragma unroll
          for (int k = 0; k < 4; ++k) v[k] = __builtin_amdgcn_raw_buffer_load_b32(rs, hb + k * 256, 0, AUX_SC1);
          const bool ok = (v[0] != SENT) & (v[1] != SENT) & (v[2] != SENT) & (v[3] != SENT);
          if (__all(ok)) break;
          __builtin_amdgcn_s_sleep(2);
        }
        if (w == 0) STAMP(l * 16 + 11);
        const float gg = rbf(__uint_as_float(v[0]) + __uint_as_float(v[2])), uu = rbf(__uint_as_float(v[1]) + __uint_as_float(v[3]));
        const float sg = rbf(1.f / (1.f + __expf(-gg)));
        const float hv = sg * uu;
        *(LAS __bf16*)(L + L_ACT + (64 * w + lane) * 2) = (__bf16)hv;
        LGKM0();
        const v4u a0 = *(const LAS v4u*)(L + L_ACT + (64 * w + 32 * (n16 & 1) + 8 * q4) * 2);
#pragma unroll
        for (int c = 0; c < 8; ++c) acc[c] = mfma(a0, wf[0][c], z4);
      } else {
#pragma unroll
        for (int c = 0; c < 8; ++c) acc[c] = z4;
      }
      put_red(acc, w);
      bar();
      if (w == 0) {
        const LAS float* rd = (const LAS float*)(L + L_RED);
        const float y = (rd[lane] + rd[64 + lane]) + (rd[128 + lane] + rd[192 + lane]);
        __builtin_amdgcn_raw_buffer_store_b32(nosent(__float_as_uint(y)), rs, WS_H5 + l * 16384 + g * 2048 + ri * 256 + lane * 4, 0, AUX_SC1);
        STAMP(l * 16 + 12);
      }
    } else {
      if (l == 0 || cu == 0) gather_x(WS_H3 + l * 16384);
      if (l == 1 && cu == 0) {
        gather_x(WS_H5 + 16384);
        if (hf == 0) *(GAS v2u*)(p.out + xcol) = v2u{pk2(xr[0], xr[1]), pk2(xr[2], xr[3])};
        if (w == 0 && lane == 0) __builtin_amdgcn_raw_buffer_store_b32(tag, rs, WS_EPOCH, 0, AUX_SC1);
        if (w == 0) STAMP(l * 16 + 13);
      }
    }
  }
}

extern "C" void kernel_launch(void* const* d_in, const int* in_sizes, int n_in, void* d_out, int out_size, void* d_ws, size_t ws_size,
                              hipStream_t stream) {
  Params p;
  p.x = (const u16*)d_in[0]; p.kc = (const u16*)d_in[1]; p.vc = (const u16*)d_in[2]; p.wq = (const u16*)d_in[3]; p.wo = (const u16*)d_in[4];
  p.wg = (const u16*)d_in[6]; p.wu = (const u16*)d_in[7]; p.wd = (const u16*)d_in[8]; p.out = (u16*)d_out; p.ws = (unsigned char*)d_ws;
  hipLaunchKernelGGL(fused_decode, dim3(256), dim3(512), 0, stream, p);
}
```

```cpp
#include <hip/hip_runtime.h>

typedef unsigned short u16;
typedef unsigned u32;
typedef unsigned long long u64;
typedef u32 v4u __attribute__((ext_vector_type(4)));
typedef u32 v2u __attribute__((ext_vector_type(2)));
typedef float v4f __attribute__((ext_vector_type(4)));
typedef float v2f __attribute__((ext_vector_type(2)));
typedef __bf16 bf16x8 __attribute__((ext_vector_type(8)));
typedef __bf16 bf16x2 __attribute__((ext_vector_type(2)));
#define GAS __attribute__((address_space(1)))
#define LAS __attribute__((address_space(3)))

constexpr int SK = 32768, WIN = 4096, FF = 1792;
constexpr u32 SENT = 0xFFFFFFFFu;
constexpr int AUX_SC1 = 16;
constexpr u32 SPIN_CAP = 200000;

constexpr int WS_EPOCH = 0;
constexpr int WS_RDY = 4096;
constexpr int WS_H1 = 8192;
constexpr int WS_H2 = 65536;
constexpr int H2_SLOT = 1152, H2_L = 256 * H2_SLOT;
constexpr int WS_H3 = WS_H2 + 2 * H2_L;
constexpr int WS_H4 = WS_H3 + 2 * 16384;
constexpr int WS_H5 = WS_H4 + 2 * 32768;
constexpr int WS_END = WS_H5 + 2 * 16384;
constexpr int WS_STAMP = 1 << 20;
constexpr int WS_BYTES = 2 << 20;

constexpr int L_QS = 0;
constexpr int L_PO = 1024;
constexpr int L_PM = 9216;
constexpr int L_ACT = 9472;
constexpr int L_ST = 10496;
constexpr int L_RED = 10560;
constexpr int LDS_BYTES = 96 * 1024;

#define D_H1 0
#define D_H2 4

struct Params {
  const u16* x; const u16* kc; const u16* vc; const u16* wq; const u16* wo;
  const u16* wg; const u16* wu; const u16* wd; u16* out; unsigned char* ws;
};

__device__ __forceinline__ float rbf(float x) { return (float)(__bf16)x; }
__device__ __forceinline__ u32 pk2(float lo, float hi) { bf16x2 v = {(__bf16)lo, (__bf16)hi}; return __builtin_bit_cast(u32, v); }
__device__ __forceinline__ float bflo(u32 w) { return __uint_as_float(w << 16); }
__device__ __forceinline__ float bfhi(u32 w) { return __uint_as_float(w & 0xffff0000u); }
__device__ __forceinline__ u32 nosent(u32 v) { return v == SENT ? 0x7FC07FC0u : v; }
__device__ __forceinline__ v4u ldnt(const void* p) { return __builtin_nontemporal_load((const GAS v4u*)p); }
__device__ __forceinline__ v4f mfma(v4u a, v4u b, v4f c) {
  return __builtin_amdgcn_mfma_f32_16x16x32_bf16(__builtin_bit_cast(bf16x8, a), __builtin_bit_cast(bf16x8, b), c, 0, 0, 0);
}
template <int CTRL> __device__ __forceinline__ float dpp(float x) {
  return __builtin_bit_cast(float, __builtin_amdgcn_mov_dpp(__builtin_bit_cast(int, x), CTRL, 0xf, 0xf, true));
}
constexpr int XOR1 = 0xB1, XOR2 = 0x4E, XOR7 = 0x141, XOR8 = 0x128, ROR4 = 0x124, XOR15 = 0x140;
__device__ __forceinline__ float x16_max(float x) {
  auto s = __builtin_amdgcn_permlane16_swap(__float_as_uint(x), __float_as_uint(x), false, false);
  x = fmaxf(__uint_as_float(s[0]), __uint_as_float(s[1]));
  auto t = __builtin_amdgcn_permlane32_swap(__float_as_uint(x), __float_as_uint(x), false, false);
  return fmaxf(__uint_as_float(t[0]), __uint_as_float(t[1]));
}
__device__ __forceinline__ float x16_sum(float x) {
  auto s = __builtin_amdgcn_permlane16_swap(__float_as_uint(x), __float_as_uint(x), false, false);
  x = __uint_as_float(s[0]) + __uint_as_float(s[1]);
  auto t = __builtin_amdgcn_permlane32_swap(__float_as_uint(x), __float_as_uint(x), false, false);
  return __uint_as_float(t[0]) + __uint_as_float(t[1]);
}
__device__ __forceinline__ float x32_sum(float x) {
  auto t = __builtin_amdgcn_permlane32_swap(__float_as_uint(x), __float_as_uint(x), false, false);
  return __uint_as_float(t[0]) + __uint_as_float(t[1]);
}
__device__ __forceinline__ float x16only_sum(float x) {
  auto s = __builtin_amdgcn_permlane16_swap(__float_as_uint(x), __float_as_uint(x), false, false);
  return __uint_as_float(s[0]) + __uint_as_float(s[1]);
}
__device__ __forceinline__ float row_sum(float x) {
  x += dpp<XOR1>(x); x += dpp<XOR2>(x); x += dpp<XOR7>(x); x += dpp<XOR15>(x);
  return x;
}
__device__ __forceinline__ void bar() { __syncthreads(); }
#define LGKM0() asm volatile("s_waitcnt lgkmcnt(0)" ::: "memory")
#define SB() __builtin_amdgcn_sched_barrier(0)

#define STAMP(id) do { } while (0)

__device__ __forceinline__ void tr8(const v4u (&r)[8], v4u (&f)[8]) {
#pragma unroll
  for (int c = 0; c < 8; ++c) {
    const u32 sel = (c & 1) ? 0x07060302u : 0x05040100u;
#pragma unroll
    for (int m = 0; m < 4; ++m) f[c][m] = __builtin_amdgcn_perm(r[2 * m + 1][c >> 1], r[2 * m][c >> 1], sel);
  }
}
__device__ __forceinline__ bool nos4(v4u v) { return (v.x != SENT) & (v.y != SENT) & (v.z != SENT) & (v.w != SENT); }

__global__ void __launch_bounds__(512, 2) fused_decode(Params p) {
  __shared__ __attribute__((aligned(16))) unsigned char lds_raw[LDS_BYTES];
  LAS unsigned char* L = (LAS unsigned char*)lds_raw;
  const int tid = threadIdx.x, lane = tid & 63, wave = __builtin_amdgcn_readfirstlane(tid >> 6);
  const int cu = blockIdx.x, g = cu & 7, j = cu >> 3, role = j >> 3, ri = j & 7;
  const int q4 = lane >> 4, n16 = lane & 15;
  const __amdgpu_buffer_rsrc_t rs = __builtin_amdgcn_make_buffer_rsrc(p.ws, 0, WS_BYTES, 0x00020000);
  const int nblk = g < 4 ? 4 : 3;
  constexpr float LOG2E = 1.4426950408889634f;

  {
    const size_t kb0 = ((size_t)g * SK + j * 128 + (wave & 3) * 32) * 128;
    const u16* src = (wave < 4 ? p.kc : p.vc) + kb0;
    v4u t[8];
#pragma unroll
    for (int e = 0; e < 8; ++e) t[e] = ldnt(src + e * 512 + lane * 8);
    u32 a = 0;
#pragma unroll
    for (int e = 0; e < 8; ++e) a |= t[e].x & t[e].y & t[e].z & t[e].w;
    asm volatile("" :: "v"(a));
    __syncthreads();
  }
  if (wave < 4) {
    STAMP(wave == 0 ? 0 : 63);
    const size_t kb = ((size_t)g * SK + (SK - WIN) + j * 128 + wave * 32) * 128;
    v4u kf[2][4], vr[8], vf[8];
#pragma unroll
    for (int t = 0; t < 2; ++t)
#pragma unroll
      for (int s = 0; s < 4; ++s) kf[t][s] = ldnt(p.kc + kb + (t * 16 + n16) * 128 + s * 32 + q4 * 8);
#pragma unroll
    for (int e = 0; e < 8; ++e) vr[e] = ldnt(p.vc + kb + (16 * (e >> 2) + 4 * q4 + (e & 3)) * 128 + n16 * 8);
    SB();
#pragma unroll
    for (int l = 0; l < 2; ++l) {
      if (role == 0) { bar(); bar(); }
      bar();
      if (wave == 0) STAMP(l * 16 + 3);
      v4u qf[4];
#pragma unroll
      for (int s = 0; s < 4; ++s) qf[s] = *(const LAS v4u*)(L + L_QS + ((n16 & 3) * 128 + s * 32 + q4 * 8) * 2);
      v4f sc[2];
#pragma unroll
      for (int t = 0; t < 2; ++t) {
        sc[t] = v4f{0.f, 0.f, 0.f, 0.f};
#pragma unroll
        for (int s = 0; s < 4; ++s) sc[t] = mfma(kf[t][s], qf[s], sc[t]);
      }
      float s8[8];
#pragma unroll
      for (int e = 0; e < 8; ++e) s8[e] = rbf(rbf(sc[e >> 2][e & 3]) * 0.08838834764831845f) * LOG2E;
      float m = fmaxf(fmaxf(fmaxf(s8[0], s8[1]), fmaxf(s8[2], s8[3])), fmaxf(fmaxf(s8[4], s8[5]), fmaxf(s8[6], s8[7])));
      m = x16_max(m);
      float lsum = 0.f;
#pragma unroll
      for (int e = 0; e < 8; ++e) { s8[e] = __builtin_amdgcn_exp2f(s8[e] - m); lsum += s8[e]; }
      lsum = x16_sum(lsum);
      v4u pa = {pk2(s8[0], s8[1]), pk2(s8[2], s8[3]), pk2(s8[4], s8[5]), pk2(s8[6], s8[7])};
      if (wave == 0) STAMP(l * 16 + 4);
      if (l == 0) tr8(vr, vf);
      v4f o[8];
#pragma unroll
      for (int c = 0; c < 8; ++c) o[c] = mfma(pa, vf[c], v4f{0.f, 0.f, 0.f, 0.f});
      {
        const v4f a = q4 == 0 ? o[0] : q4 == 1 ? o[2] : q4 == 2 ? o[4] : o[6];
        const v4f b = q4 == 0 ? o[1] : q4 == 1 ? o[3] : q4 == 2 ? o[5] : o[7];
#pragma unroll
        for (int r = 0; r < 4; ++r) *(LAS v2f*)(L + L_PO + ((wave * 4 + r) * 128 + n16 * 8 + q4 * 2) * 4) = v2f{a[r], b[r]};
        if (lane < 4) *(LAS v2f*)(L + L_PM + (lane * 4 + wave) * 8) = v2f{m, lsum};
      }
      bar();
      if (wave == 0) STAMP(l * 16 + 5);
      {
        const v4f ml0 = *(const LAS v4f*)(L + L_PM + wave * 32), ml1 = *(const LAS v4f*)(L + L_PM + wave * 32 + 16);
        v2f pv[4];
#pragma unroll
        for (int w = 0; w < 4; ++w) pv[w] = *(const LAS v2f*)(L + L_PO + ((w * 4 + wave) * 128 + lane * 2) * 4);
        const float M = fmaxf(fmaxf(ml0[0], ml0[2]), fmaxf(ml1[0], ml1[2]));
        const float s0 = __builtin_amdgcn_exp2f(ml0[0] - M), s1 = __builtin_amdgcn_exp2f(ml0[2] - M);
        const float s2 = __builtin_amdgcn_exp2f(ml1[0] - M), s3 = __builtin_amdgcn_exp2f(ml1[2] - M);
        const float Ls = (s0 * ml0[1] + s1 * ml0[3]) + (s2 * ml1[1] + s3 * ml1[3]);
        const float inv = __builtin_amdgcn_rcpf(Ls);
        const float o0 = ((s0 * pv[0][0] + s1 * pv[1][0]) + (s2 * pv[2][0] + s3 * pv[3][0])) * inv;
        const float o1 = ((s0 * pv[0][1] + s1 * pv[1][1]) + (s2 * pv[2][1] + s3 * pv[3][1])) * inv;
        const int slot = WS_H2 + l * H2_L + (g * 32 + j) * H2_SLOT;
        __builtin_amdgcn_raw_buffer_store_b32(nosent(pk2(o0, o1)), rs, slot + wave * 256 + lane * 4, 0, AUX_SC1);
        if (lane == 0) __builtin_amdgcn_raw_buffer_store_b32(nosent(__float_as_uint(M + __builtin_amdgcn_logf(Ls))), rs, slot + 1024 + wave * 4, 0, AUX_SC1);
      }
      if (wave == 0) STAMP(l * 16 + 6);
      if (role == 1 || role == 3) bar();
      if (role == 2) { bar(); bar(); }
    }
    return;
  }

  const int w = wave - 4;
  const int kr = n16 >> 3, cb = n16 & 7;
  if (w == 3) {
    const v4u sv = {SENT, SENT, SENT, SENT};
#pragma unroll
    for (int l = 0; l < 2; ++l) {
      const int slot = WS_H2 + l * H2_L + (g * 32 + j) * H2_SLOT;
      __builtin_amdgcn_raw_buffer_store_b128(sv, rs, slot + lane * 16, 0, AUX_SC1);
      int off = slot + 1024;
      bool on = lane == 63;
      if (role == 1) { if (lane < 16) { off = WS_H3 + l * 16384 + g * 2048 + ri * 256 + lane * 16; on = true; } }
      if (role == 2) { if (lane < 32) { off = WS_H4 + l * 32768 + g * 4096 + ri * 512 + lane * 16; on = true; } }
      if (role == 3) { if (lane < 16) { off = WS_H5 + l * 16384 + g * 2048 + ri * 256 + lane * 16; on = true; } }
      if (on) __builtin_amdgcn_raw_buffer_store_b128(sv, rs, off, 0, AUX_SC1);
    }
  }
  const u32 ep_raw = __builtin_amdgcn_raw_buffer_load_b32(rs, WS_EPOCH, 0, AUX_SC1);

  const u16* wsrc[2]; int pitch; bool wact = true;
  {
    const int rl = 32 * kr + 8 * q4;
    if (role == 0) { pitch = 4096; wsrc[0] = p.wq + (size_t)(128 * w + rl) * 4096 + g * 512 + ri * 64 + cb * 8; wsrc[1] = wsrc[0] + 64 * 4096; }
    else if (role == 1) { pitch = 512; wsrc[0] = p.wo + (size_t)(g * 512 + 128 * w + rl) * 512 + ri * 64 + cb * 8; wsrc[1] = wsrc[0] + 64 * 512; }
    else if (role == 2) {
      const int bi = ri >> 1, kh = ri & 1; wact = bi < nblk; const int blk = wact ? g + 8 * bi : g;
      pitch = FF; const size_t o = (size_t)(256 * kh + 64 * w + rl) * FF + blk * 64 + cb * 8; wsrc[0] = p.wg + o; wsrc[1] = p.wu + o;
    } else {
      wact = w < nblk; const int blk = wact ? g + 8 * w : g;
      pitch = 512; wsrc[0] = p.wd + (size_t)(blk * 64 + rl) * 512 + ri * 64 + cb * 8; wsrc[1] = wsrc[0];
    }
  }
  v4u wr[2][8], wf[2][8];
  auto issue_w = [&]() {
#pragma unroll
    for (int e = 0; e < 8; ++e) wr[0][e] = ldnt(wsrc[0] + (size_t)e * pitch);
    if (role != 3) {
#pragma unroll
      for (int e = 0; e < 8; ++e) wr[1][e] = ldnt(wsrc[1] + (size_t)e * pitch);
    } else {
#pragma unroll
      for (int e = 0; e < 8; ++e) wr[1][e] = v4u{0u, 0u, 0u, 0u};
    }
    SB();
  };
  const int l31 = lane & 31, hf = lane >> 5;
  int xcol;
  if (role == 2) { const int kh = ri & 1; xcol = (l31 < 16) ? 256 * kh + 64 * w + 4 * l31 : 256 * (1 - kh) + 64 * w + 4 * (l31 - 16); }
  else xcol = 128 * w + 4 * l31;
  const v2u xv = *(const GAS v2u*)(p.x + xcol);
  SB();
  if (role == 0 || w != 3) issue_w();
  SB();
  float xr[4];
  xr[0] = bflo(xv.x); xr[1] = bfhi(xv.x); xr[2] = bflo(xv.y); xr[3] = bfhi(xv.y);
  const u32 tag = __builtin_amdgcn_readfirstlane(ep_raw) + 1u;
  if (w == 3) {
    asm volatile("s_waitcnt vmcnt(0)" ::: "memory");
    if (lane == 0) __builtin_amdgcn_raw_buffer_store_b64(v2u{tag, tag}, rs, WS_RDY + cu * 8, 0, AUX_SC1);
  }

  auto gather_x = [&](int base) {
    v4u v[4];
    for (u32 spins = 0; spins < SPIN_CAP; ++spins) {
#pragma unroll
      for (int k = 0; k < 4; ++k) v[k] = __builtin_amdgcn_raw_buffer_load_b128(rs, base + (hf * 4 + k) * 2048 + xcol * 4, 0, AUX_SC1);
      const bool ok = nos4(v[0]) & nos4(v[1]) & nos4(v[2]) & nos4(v[3]);
      if (__all(ok)) break;
      __builtin_amdgcn_s_sleep(2);
    }
#pragma unroll
    for (int c = 0; c < 4; ++c) {
      float s = (__uint_as_float(v[0][c]) + __uint_as_float(v[1][c])) + (__uint_as_float(v[2][c]) + __uint_as_float(v[3][c]));
      s = x32_sum(s);
      xr[c] = rbf(xr[c] + rbf(s));
    }
  };
  auto layernorm = [&]() {
    float s = (xr[0] + xr[1]) + (xr[2] + xr[3]);
    s = x16only_sum(row_sum(s));
    const float mw = s * (1.f / 128.f);
    float d0 = xr[0] - mw, d1 = xr[1] - mw, d2 = xr[2] - mw, d3 = xr[3] - mw;
    float m2 = (d0 * d0 + d1 * d1) + (d2 * d2 + d3 * d3);
    m2 = x16only_sum(row_sum(m2));
    if (lane == 0) *(LAS v2f*)(L + L_ST + w * 8) = v2f{mw, m2};
    bar();
    const v4f a = *(const LAS v4f*)(L + L_ST), b = *(const LAS v4f*)(L + L_ST + 16);
    const float mean = ((a[0] + a[2]) + (b[0] + b[2])) * 0.25f;
    const float e0 = a[0] - mean, e1 = a[2] - mean, e2 = b[0] - mean, e3 = b[2] - mean;
    const float M2 = ((a[1] + a[3]) + (b[1] + b[3])) + 128.f * ((e0 * e0 + e1 * e1) + (e2 * e2 + e3 * e3));
    const float var = rbf(M2 * (1.f / 512.f));
    const float sd = rbf(__builtin_sqrtf(rbf(var + 1e-5f)));
    const float mb = rbf(mean);
    const float inv = 1.f / sd;
    v2u o;
    o.x = pk2(rbf(xr[0] - mb) * inv, rbf(xr[1] - mb) * inv);
    o.y = pk2(rbf(xr[2] - mb) * inv, rbf(xr[3] - mb) * inv);
    if (hf == 0) *(LAS v2u*)(L + L_ACT + xcol * 2) = o;
    LGKM0();
  };
  auto put_red = [&](const v4f (&acc)[8], int slot) {
    float v[8];
#pragma unroll
    for (int c = 0; c < 8; ++c) { const float t = kr ? acc[c][1] : acc[c][0]; v[c] = t + dpp<XOR8>(t); }
    if (lane < 8) {
      *(LAS v4f*)(L + L_RED + (slot * 64 + cb * 8) * 4) = v4f{v[0], v[1], v[2], v[3]};
      *(LAS v4f*)(L + L_RED + (slot * 64 + cb * 8 + 4) * 4) = v4f{v[4], v[5], v[6], v[7]};
    }
  };
  const v4f z4 = {0.f, 0.f, 0.f, 0.f};

#pragma unroll
  for (int l = 0; l < 2; ++l) {
    if (role == 0) {
      if (l == 0) { tr8(wr[0], wf[0]); tr8(wr[1], wf[1]); }
      else gather_x(WS_H5);
      if (w == 0) STAMP(l * 16 + 1);
      layernorm();
      v4f acc[8];
      const v4u a0 = *(const LAS v4u*)(L + L_ACT + (128 * w + 32 * (n16 & 1) + 8 * q4) * 2);
      const v4u a1 = *(const LAS v4u*)(L + L_ACT + (128 * w + 64 + 32 * (n16 & 1) + 8 * q4) * 2);
#pragma unroll
      for (int c = 0; c < 8; ++c) acc[c] = mfma(a1, wf[1][c], mfma(a0, wf[0][c], z4));
      put_red(acc, w);
      bar();
      if (w == 0) {
        const LAS float* rd = (const LAS float*)(L + L_RED);
        const float qv = (rd[lane] + rd[64 + lane]) + (rd[128 + lane] + rd[192 + lane]);
        const float qn = dpp<XOR1>(qv);
        if ((lane & 1) == 0) __builtin_amdgcn_raw_buffer_store_b64(v2u{pk2(qv, qn), tag}, rs, WS_H1 + l * 16384 + g * 2048 + ri * 256 + lane * 4, 0, AUX_SC1);
        STAMP(l * 16 + 2);
      }
    }
    if (w == 3) {
      if (D_H1 && (l == 1 || role == 0)) __builtin_amdgcn_s_sleep(D_H1);
      v4u v[2];
      for (u32 spins = 0; spins < SPIN_CAP; ++spins) {
#pragma unroll
        for (int k = 0; k < 2; ++k) v[k] = __builtin_amdgcn_raw_buffer_load_b128(rs, WS_H1 + l * 16384 + g * 2048 + k * 1024 + lane * 16, 0, AUX_SC1);
        const bool ok = (v[0].y == tag) & (v[0].w == tag) & (v[1].y == tag) & (v[1].w == tag);
        if (__all(ok)) break;
        __builtin_amdgcn_s_sleep(2);
      }
#pragma unroll
      for (int k = 0; k < 2; ++k) *(LAS v2u*)(L + L_QS + k * 512 + lane * 8) = v2u{v[k].x, v[k].z};
    }
    bar();
    if (l == 0) {
      if (w == 3) {
        v4u v[2];
        for (u32 spins = 0; spins < SPIN_CAP; ++spins) {
#pragma unroll
          for (int k = 0; k < 2; ++k) v[k] = __builtin_amdgcn_raw_buffer_load_b128(rs, WS_RDY + k * 1024 + lane * 16, 0, AUX_SC1);
          const bool ok = (v[0].x == tag) & (v[0].z == tag) & (v[1].x == tag) & (v[1].z == tag);
          if (__all(ok)) break;
          __builtin_amdgcn_s_sleep(2);
        }
        if (role != 0) issue_w();
      }
    }
    bar();
    if (l == 0 && role != 0) { tr8(wr[0], wf[0]); if (role != 3) tr8(wr[1], wf[1]); }
    if (role == 1) {
      if (D_H2) __builtin_amdgcn_s_sleep(D_H2);
      v4u pr[8]; float lse[8];
      const int hb = WS_H2 + l * H2_L + (g * 32 + 8 * q4) * H2_SLOT;
      for (u32 spins = 0; spins < SPIN_CAP; ++spins) {
        bool ok = true;
#pragma unroll
        for (int e = 0; e < 8; ++e) lse[e] = __uint_as_float(__builtin_amdgcn_raw_buffer_load_b32(rs, hb + e * H2_SLOT + 1024 + w * 4, 0, AUX_SC1));
#pragma unroll
        for (int e = 0; e < 8; ++e) pr[e] = __builtin_amdgcn_raw_buffer_load_b128(rs, hb + e * H2_SLOT + w * 256 + n16 * 16, 0, AUX_SC1);
#pragma unroll
        for (int e = 0; e < 8; ++e) ok &= nos4(pr[e]) & (__float_as_uint(lse[e]) != SENT);
        if (__all(ok)) break;
        __builtin_amdgcn_s_sleep(2);
      }
      if (w == 0) STAMP(l * 16 + 7);
      float M = fmaxf(fmaxf(fmaxf(lse[0], lse[1]), fmaxf(lse[2], lse[3])), fmaxf(fmaxf(lse[4], lse[5]), fmaxf(lse[6], lse[7])));
      M = x16_max(M);
      float Ls = 0.f;
#pragma unroll
      for (int e = 0; e < 8; ++e) { lse[e] = __builtin_amdgcn_exp2f(lse[e] - M); Ls += lse[e]; }
      Ls = x16_sum(Ls);
      const float inv = __builtin_amdgcn_rcpf(Ls);
#pragma unroll
      for (int e = 0; e < 8; ++e) { const float wv = lse[e] * inv, hi = rbf(wv); lse[e] = (n16 & 1) ? wv - hi : hi; }
      const v4u wa = {pk2(lse[0], lse[1]), pk2(lse[2], lse[3]), pk2(lse[4], lse[5]), pk2(lse[6], lse[7])};
      v4u pf[8];
      tr8(pr, pf);
      float ov[8];
#pragma unroll
      for (int c = 0; c < 8; ++c) { const v4f d = mfma(wa, pf[c], z4); ov[c] = d[0] + d[1]; }
      if (q4 == 0) *(LAS v4u*)(L + L_ACT + (128 * w + 8 * n16) * 2) = v4u{pk2(ov[0], ov[1]), pk2(ov[2], ov[3]), pk2(ov[4], ov[5]), pk2(ov[6], ov[7])};
      LGKM0();
      v4f acc[8];
      const v4u a0 = *(const LAS v4u*)(L + L_ACT + (128 * w + 32 * (n16 & 1) + 8 * q4) * 2);
      const v4u a1 = *(const LAS v4u*)(L + L_ACT + (128 * w + 64 + 32 * (n16 & 1) + 8 * q4) * 2);
#pragma unroll
      for (int c = 0; c < 8; ++c) acc[c] = mfma(a1, wf[1][c], mfma(a0, wf[0][c], z4));
      put_red(acc, w);
      bar();
      if (w == 0) {
        const LAS float* rd = (const LAS float*)(L + L_RED);
        const float y = (rd[lane] + rd[64 + lane]) + (rd[128 + lane] + rd[192 + lane]);
        __builtin_amdgcn_raw_buffer_store_b32(nosent(__float_as_uint(y)), rs, WS_H3 + l * 16384 + g * 2048 + ri * 256 + lane * 4, 0, AUX_SC1);
        STAMP(l * 16 + 8);
      }
    } else if (role == 2) {
      if (l == 1) gather_x(WS_H5);
      gather_x(WS_H3 + l * 16384);
      if (w == 0) STAMP(l * 16 + 9);
      layernorm();
      const int kh = ri & 1;
      const v4u a0 = *(const LAS v4u*)(L + L_ACT + (256 * kh + 64 * w + 32 * (n16 & 1) + 8 * q4) * 2);
      v4f acc[8];
#pragma unroll
      for (int c = 0; c < 8; ++c) acc[c] = mfma(a0, wf[0][c], z4);
      put_red(acc, w);
#pragma unroll
      for (int c = 0; c < 8; ++c) acc[c] = mfma(a0, wf[1][c], z4);
      put_red(acc, 4 + w);
      bar();
      if (w < 2 && wact) {
        const LAS float* rd = (const LAS float*)(L + L_RED) + w * 256;
        const float y = (rd[lane] + rd[64 + lane]) + (rd[128 + lane] + rd[192 + lane]);
        __builtin_amdgcn_raw_buffer_store_b32(nosent(__float_as_uint(y)), rs, WS_H4 + l * 32768 + g * 4096 + ri * 512 + w * 256 + lane * 4, 0, AUX_SC1);
      }
      if (w == 0) STAMP(l * 16 + 10);
    } else if (role == 3) {
      v4f acc[8];
      if (wact) {
        u32 v[4];
        const int hb = WS_H4 + l * 32768 + g * 4096 + w * 1024 + lane * 4;
        for (u32 spins = 0; spins < SPIN_CAP; ++spins) {
#pragma unroll
          for (int k = 0; k < 4; ++k) v[k] = __builtin_amdgcn_raw_buffer_load_b32(rs, hb + k * 256, 0, AUX_SC1);
          const bool ok = (v[0] != SENT) & (v[1] != SENT) & (v[2] != SENT) & (v[3] != SENT);
          if (__all(ok)) break;
          __builtin_amdgcn_s_sleep(2);
        }
        if (w == 0) STAMP(l * 16 + 11);
        const float gg = rbf(__uint_as_float(v[0]) + __uint_as_float(v[2])), uu = rbf(__uint_as_float(v[1]) + __uint_as_float(v[3]));
        const float sg = rbf(1.f / (1.f + __expf(-gg)));
        const float hv = sg * uu;
        *(LAS __bf16*)(L + L_ACT + (64 * w + lane) * 2) = (__bf16)hv;
        LGKM0();
        const v4u a0 = *(const LAS v4u*)(L + L_ACT + (64 * w + 32 * (n16 & 1) + 8 * q4) * 2);
#pragma unroll
        for (int c = 0; c < 8; ++c) acc[c] = mfma(a0, wf[0][c], z4);
      } else {
#pragma unroll
        for (int c = 0; c < 8; ++c) acc[c] = z4;
      }
      put_red(acc, w);
      bar();
      if (w == 0) {
        const LAS float* rd = (const LAS float*)(L + L_RED);
        const float y = (rd[lane] + rd[64 + lane]) + (rd[128 + lane] + rd[192 + lane]);
        __builtin_amdgcn_raw_buffer_store_b32(nosent(__float_as_uint(y)), rs, WS_H5 + l * 16384 + g * 2048 + ri * 256 + lane * 4, 0, AUX_SC1);
        STAMP(l * 16 + 12);
      }
    } else {
      if (l == 0 || cu == 0) gather_x(WS_H3 + l * 16384);
      if (l == 1 && cu == 0) {
        gather_x(WS_H5 + 16384);
        if (hf == 0) *(GAS v2u*)(p.out + xcol) = v2u{pk2(xr[0], xr[1]), pk2(xr[2], xr[3])};
        if (w == 0 && lane == 0) __builtin_amdgcn_raw_buffer_store_b32(tag, rs, WS_EPOCH, 0, AUX_SC1);
        if (w == 0) STAMP(l * 16 + 13);
      }
    }
  }
}

extern "C" void kernel_launch(void* const* d_in, const int* in_sizes, int n_in, void* d_out, int out_size, void* d_ws, size_t ws_size,
                              hipStream_t stream) {
  Params p;
  p.x = (const u16*)d_in[0]; p.kc = (const u16*)d_in[1]; p.vc = (const u16*)d_in[2]; p.wq = (const u16*)d_in[3]; p.wo = (const u16*)d_in[4];
  p.wg = (const u16*)d_in[6]; p.wu = (const u16*)d_in[7]; p.wd = (const u16*)d_in[8]; p.out = (u16*)d_out; p.ws = (unsigned char*)d_ws;
  hipLaunchKernelGGL(fused_decode, dim3(256), dim3(512), 0, stream, p);
}
```

```cpp
#include <hip/hip_runtime.h>

typedef unsigned short u16;
typedef unsigned u32;
typedef unsigned long long u64;
typedef u32 v4u __attribute__((ext_vector_type(4)));
typedef u32 v2u __attribute__((ext_vector_type(2)));
typedef float v4f __attribute__((ext_vector_type(4)));
typedef float v2f __attribute__((ext_vector_type(2)));
typedef __bf16 bf16x8 __attribute__((ext_vector_type(8)));
typedef __bf16 bf16x2 __attribute__((ext_vector_type(2)));
#define GAS __attribute__((address_space(1)))
#define LAS __attribute__((address_space(3)))

constexpr int SK = 32768, WIN = 4096, FF = 1792;
constexpr u32 SENT = 0xFFFFFFFFu;
constexpr int AUX_SC1 = 16;
constexpr u32 SPIN_CAP = 200000;

constexpr int WS_EPOCH = 0;
constexpr int WS_RDY = 4096;
constexpr int WS_H1 = 8192;
constexpr int WS_H2 = 65536;
constexpr int H2_SLOT = 1152, H2_L = 256 * H2_SLOT;
constexpr int WS_H3 = WS_H2 + 2 * H2_L;
constexpr int WS_H4 = WS_H3 + 2 * 16384;
constexpr int WS_H5 = WS_H4 + 2 * 32768;
constexpr int WS_END = WS_H5 + 2 * 16384;
constexpr int WS_STAMP = 1 << 20;
constexpr int WS_BYTES = 2 << 20;

constexpr int L_QS = 0;
constexpr int L_PO = 1024;
constexpr int L_PM = 9216;
constexpr int L_ACT = 9472;
constexpr int L_ST = 10496;
constexpr int L_RED = 10560;
constexpr int LDS_BYTES = 96 * 1024;

#define D_H1 0
#define D_H2 4

struct Params {
  const u16* x; const u16* kc; const u16* vc; const u16* wq; const u16* wo;
  const u16* wg; const u16* wu; const u16* wd; u16* out; unsigned char* ws;
};

__device__ __forceinline__ float rbf(float x) { return (float)(__bf16)x; }
__device__ __forceinline__ u32 pk2(float lo, float hi) { bf16x2 v = {(__bf16)lo, (__bf16)hi}; return __builtin_bit_cast(u32, v); }
__device__ __forceinline__ float bflo(u32 w) { return __uint_as_float(w << 16); }
__device__ __forceinline__ float bfhi(u32 w) { return __uint_as_float(w & 0xffff0000u); }
__device__ __forceinline__ u32 nosent(u32 v) { return v == SENT ? 0x7FC07FC0u : v; }
__device__ __forceinline__ v4u ldnt(const void* p) { return __builtin_nontemporal_load((const GAS v4u*)p); }
__device__ __forceinline__ v4f mfma(v4u a, v4u b, v4f c) {
  return __builtin_amdgcn_mfma_f32_16x16x32_bf16(__builtin_bit_cast(bf16x8, a), __builtin_bit_cast(bf16x8, b), c, 0, 0, 0);
}
template <int CTRL> __device__ __forceinline__ float dpp(float x) {
  return __builtin_bit_cast(float, __builtin_amdgcn_mov_dpp(__builtin_bit_cast(int, x), CTRL, 0xf, 0xf, true));
}
constexpr int XOR1 = 0xB1, XOR2 = 0x4E, XOR7 = 0x141, XOR8 = 0x128, ROR4 = 0x124, XOR15 = 0x140;
__device__ __forceinline__ float x16_max(float x) {
  auto s = __builtin_amdgcn_permlane16_swap(__float_as_uint(x), __float_as_uint(x), false, false);
  x = fmaxf(__uint_as_float(s[0]), __uint_as_float(s[1]));
  auto t = __builtin_amdgcn_permlane32_swap(__float_as_uint(x), __float_as_uint(x), false, false);
  return fmaxf(__uint_as_float(t[0]), __uint_as_float(t[1]));
}
__device__ __forceinline__ float x16_sum(float x) {
  auto s = __builtin_amdgcn_permlane16_swap(__float_as_uint(x), __float_as_uint(x), false, false);
  x = __uint_as_float(s[0]) + __uint_as_float(s[1]);
  auto t = __builtin_amdgcn_permlane32_swap(__float_as_uint(x), __float_as_uint(x), false, false);
  return __uint_as_float(t[0]) + __uint_as_float(t[1]);
}
__device__ __forceinline__ float x32_sum(float x) {
  auto t = __builtin_amdgcn_permlane32_swap(__float_as_uint(x), __float_as_uint(x), false, false);
  return __uint_as_float(t[0]) + __uint_as_float(t[1]);
}
__device__ __forceinline__ float x16only_sum(float x) {
  auto s = __builtin_amdgcn_permlane16_swap(__float_as_uint(x), __float_as_uint(x), false, false);
  return __uint_as_float(s[0]) + __uint_as_float(s[1]);
}
__device__ __forceinline__ float row_sum(float x) {
  x += dpp<XOR1>(x); x += dpp<XOR2>(x); x += dpp<XOR7>(x); x += dpp<XOR15>(x);
  return x;
}
__device__ __forceinline__ void bar() { __syncthreads(); }
#define LGKM0() asm volatile("s_waitcnt lgkmcnt(0)" ::: "memory")
#define SB() __builtin_amdgcn_sched_barrier(0)

#define STAMP(id) do { } while (0)

__device__ __forceinline__ void tr8(const v4u (&r)[8], v4u (&f)[8]) {
#pragma unroll
  for (int c = 0; c < 8; ++c) {
    const u32 sel = (c & 1) ? 0x07060302u : 0x05040100u;
#pragma unroll
    for (int m = 0; m < 4; ++m) f[c][m] = __builtin_amdgcn_perm(r[2 * m + 1][c >> 1], r[2 * m][c >> 1], sel);
  }
}
__device__ __forceinline__ bool nos4(v4u v) { return (v.x != SENT) & (v.y != SENT) & (v.z != SENT) & (v.w != SENT); }

__global__ void __launch_bounds__(512, 2) fused_decode(Params p) {
  __shared__ __attribute__((aligned(16))) unsigned char lds_raw[LDS_BYTES];
  LAS unsigned char* L = (LAS unsigned char*)lds_raw;
  const u64 t_entry = __builtin_amdgcn_s_memrealtime();
  const int tid = threadIdx.x, lane = tid & 63, wave = __builtin_amdgcn_readfirstlane(tid >> 6);
  const int cu = blockIdx.x, g = cu & 7, j = cu >> 3, role = j >> 3, ri = j & 7;
  const int q4 = lane >> 4, n16 = lane & 15;
  const __amdgpu_buffer_rsrc_t rs = __builtin_amdgcn_make_buffer_rsrc(p.ws, 0, WS_BYTES, 0x00020000);
  const int nblk = g < 4 ? 4 : 3;
  constexpr float LOG2E = 1.4426950408889634f;

  if (wave < 4) {
    STAMP(wave == 0 ? 0 : 63);
    const size_t kb = ((size_t)g * SK + (SK - WIN) + j * 128 + wave * 32) * 128;
    v4u kf[2][4], vr[8], vf[8];
#pragma unroll
    for (int t = 0; t < 2; ++t)
#pragma unroll
      for (int s = 0; s < 4; ++s) kf[t][s] = ldnt(p.kc + kb + (t * 16 + n16) * 128 + s * 32 + q4 * 8);
#pragma unroll
    for (int e = 0; e < 8; ++e) vr[e] = ldnt(p.vc + kb + (16 * (e >> 2) + 4 * q4 + (e & 3)) * 128 + n16 * 8);
    SB();
#pragma unroll
    for (int l = 0; l < 2; ++l) {
      if (role == 0) { bar(); bar(); }
      bar();
      if (wave == 0) STAMP(l * 16 + 3);
      v4u qf[4];
#pragma unroll
      for (int s = 0; s < 4; ++s) qf[s] = *(const LAS v4u*)(L + L_QS + ((n16 & 3) * 128 + s * 32 + q4 * 8) * 2);
      v4f sc[2];
#pragma unroll
      for (int t = 0; t < 2; ++t) {
        sc[t] = v4f{0.f, 0.f, 0.f, 0.f};
#pragma unroll
        for (int s = 0; s < 4; ++s) sc[t] = mfma(kf[t][s], qf[s], sc[t]);
      }
      float s8[8];
#pragma unroll
      for (int e = 0; e < 8; ++e) s8[e] = rbf(rbf(sc[e >> 2][e & 3]) * 0.08838834764831845f) * LOG2E;
      float m = fmaxf(fmaxf(fmaxf(s8[0], s8[1]), fmaxf(s8[2], s8[3])), fmaxf(fmaxf(s8[4], s8[5]), fmaxf(s8[6], s8[7])));
      m = x16_max(m);
      float lsum = 0.f;
#pragma unroll
      for (int e = 0; e < 8; ++e) { s8[e] = __builtin_amdgcn_exp2f(s8[e] - m); lsum += s8[e]; }
      lsum = x16_sum(lsum);
      v4u pa = {pk2(s8[0], s8[1]), pk2(s8[2], s8[3]), pk2(s8[4], s8[5]), pk2(s8[6], s8[7])};
      if (wave == 0) STAMP(l * 16 + 4);
      if (l == 0) tr8(vr, vf);
      v4f o[8];
#pragma unroll
      for (int c = 0; c < 8; ++c) o[c] = mfma(pa, vf[c], v4f{0.f, 0.f, 0.f, 0.f});
      {
        const v4f a = q4 == 0 ? o[0] : q4 == 1 ? o[2] : q4 == 2 ? o[4] : o[6];
        const v4f b = q4 == 0 ? o[1] : q4 == 1 ? o[3] : q4 == 2 ? o[5] : o[7];
#pragma unroll
        for (int r = 0; r < 4; ++r) *(LAS v2f*)(L + L_PO + ((wave * 4 + r) * 128 + n16 * 8 + q4 * 2) * 4) = v2f{a[r], b[r]};
        if (lane < 4) *(LAS v2f*)(L + L_PM + (lane * 4 + wave) * 8) = v2f{m, lsum};
      }
      bar();
      if (wave == 0) STAMP(l * 16 + 5);
      {
        const v4f ml0 = *(const LAS v4f*)(L + L_PM + wave * 32), ml1 = *(const LAS v4f*)(L + L_PM + wave * 32 + 16);
        v2f pv[4];
#pragma unroll
        for (int w = 0; w < 4; ++w) pv[w] = *(const LAS v2f*)(L + L_PO + ((w * 4 + wave) * 128 + lane * 2) * 4);
        const float M = fmaxf(fmaxf(ml0[0], ml0[2]), fmaxf(ml1[0], ml1[2]));
        const float s0 = __builtin_amdgcn_exp2f(ml0[0] - M), s1 = __builtin_amdgcn_exp2f(ml0[2] - M);
        const float s2 = __builtin_amdgcn_exp2f(ml1[0] - M), s3 = __builtin_amdgcn_exp2f(ml1[2] - M);
        const float Ls = (s0 * ml0[1] + s1 * ml0[3]) + (s2 * ml1[1] + s3 * ml1[3]);
        const float inv = __builtin_amdgcn_rcpf(Ls);
        const float o0 = ((s0 * pv[0][0] + s1 * pv[1][0]) + (s2 * pv[2][0] + s3 * pv[3][0])) * inv;
        const float o1 = ((s0 * pv[0][1] + s1 * pv[1][1]) + (s2 * pv[2][1] + s3 * pv[3][1])) * inv;
        const int slot = WS_H2 + l * H2_L + (g * 32 + j) * H2_SLOT;
        __builtin_amdgcn_raw_buffer_store_b32(nosent(pk2(o0, o1)), rs, slot + wave * 256 + lane * 4, 0, AUX_SC1);
        if (lane == 0) __builtin_amdgcn_raw_buffer_store_b32(nosent(__float_as_uint(M + __builtin_amdgcn_logf(Ls))), rs, slot + 1024 + wave * 4, 0, AUX_SC1);
      }
      if (wave == 0) STAMP(l * 16 + 6);
      if (role == 1 || role == 3) bar();
      if (role == 2) { bar(); bar(); }
    }
    return;
  }

  const int w = wave - 4;
  const int kr = n16 >> 3, cb = n16 & 7;
  if (w == 3) {
    const v4u sv = {SENT, SENT, SENT, SENT};
#pragma unroll
    for (int l = 0; l < 2; ++l) {
      const int slot = WS_H2 + l * H2_L + (g * 32 + j) * H2_SLOT;
      __builtin_amdgcn_raw_buffer_store_b128(sv, rs, slot + lane * 16, 0, AUX_SC1);
      int off = slot + 1024;
      bool on = lane == 63;
      if (role == 1) { if (lane < 16) { off = WS_H3 + l * 16384 + g * 2048 + ri * 256 + lane * 16; on = true; } }
      if (role == 2) { if (lane < 32) { off = WS_H4 + l * 32768 + g * 4096 + ri * 512 + lane * 16; on = true; } }
      if (role == 3) { if (lane < 16) { off = WS_H5 + l * 16384 + g * 2048 + ri * 256 + lane * 16; on = true; } }
      if (on) __builtin_amdgcn_raw_buffer_store_b128(sv, rs, off, 0, AUX_SC1);
    }
  }
  const u32 ep_raw = __builtin_amdgcn_raw_buffer_load_b32(rs, WS_EPOCH, 0, AUX_SC1);

  const u16* wsrc[2]; int pitch; bool wact = true;
  {
    const int rl = 32 * kr + 8 * q4;
    if (role == 0) { pitch = 4096; wsrc[0] = p.wq + (size_t)(128 * w + rl) * 4096 + g * 512 + ri * 64 + cb * 8; wsrc[1] = wsrc[0] + 64 * 4096; }
    else if (role == 1) { pitch = 512; wsrc[0] = p.wo + (size_t)(g * 512 + 128 * w + rl) * 512 + ri * 64 + cb * 8; wsrc[1] = wsrc[0] + 64 * 512; }
    else if (role == 2) {
      const int bi = ri >> 1, kh = ri & 1; wact = bi < nblk; const int blk = wact ? g + 8 * bi : g;
      pitch = FF; const size_t o = (size_t)(256 * kh + 64 * w + rl) * FF + blk * 64 + cb * 8; wsrc[0] = p.wg + o; wsrc[1] = p.wu + o;
    } else {
      wact = w < nblk; const int blk = wact ? g + 8 * w : g;
      pitch = 512; wsrc[0] = p.wd + (size_t)(blk * 64 + rl) * 512 + ri * 64 + cb * 8; wsrc[1] = wsrc[0];
    }
  }
  v4u wr[2][8], wf[2][8];
  auto issue_w = [&]() {
#pragma unroll
    for (int e = 0; e < 8; ++e) wr[0][e] = ldnt(wsrc[0] + (size_t)e * pitch);
    if (role != 3) {
#pragma unroll
      for (int e = 0; e < 8; ++e) wr[1][e] = ldnt(wsrc[1] + (size_t)e * pitch);
    } else {
#pragma unroll
      for (int e = 0; e < 8; ++e) wr[1][e] = v4u{0u, 0u, 0u, 0u};
    }
    SB();
  };
  const int l31 = lane & 31, hf = lane >> 5;
  int xcol;
  if (role == 2) { const int kh = ri & 1; xcol = (l31 < 16) ? 256 * kh + 64 * w + 4 * l31 : 256 * (1 - kh) + 64 * w + 4 * (l31 - 16); }
  else xcol = 128 * w + 4 * l31;
  const v2u xv = *(const GAS v2u*)(p.x + xcol);
  SB();
  if (role == 0 || w != 3) issue_w();
  SB();
  float xr[4];
  xr[0] = bflo(xv.x); xr[1] = bfhi(xv.x); xr[2] = bflo(xv.y); xr[3] = bfhi(xv.y);
  const u32 tag = __builtin_amdgcn_readfirstlane(ep_raw) + 1u;
  if (w == 3) {
    asm volatile("s_waitcnt vmcnt(0)" ::: "memory");
    if (lane == 0) __builtin_amdgcn_raw_buffer_store_b64(v2u{tag, tag}, rs, WS_RDY + cu * 8, 0, AUX_SC1);
  }

  auto gather_x = [&](int base) {
    v4u v[4];
    for (u32 spins = 0; spins < SPIN_CAP; ++spins) {
#pragma unroll
      for (int k = 0; k < 4; ++k) v[k] = __builtin_amdgcn_raw_buffer_load_b128(rs, base + (hf * 4 + k) * 2048 + xcol * 4, 0, AUX_SC1);
      const bool ok = nos4(v[0]) & nos4(v[1]) & nos4(v[2]) & nos4(v[3]);
      if (__all(ok)) break;
      __builtin_amdgcn_s_sleep(2);
    }
#pragma unroll
    for (int c = 0; c < 4; ++c) {
      float s = (__uint_as_float(v[0][c]) + __uint_as_float(v[1][c])) + (__uint_as_float(v[2][c]) + __uint_as_float(v[3][c]));
      s = x32_sum(s);
      xr[c] = rbf(xr[c] + rbf(s));
    }
  };
  auto layernorm = [&]() {
    float s = (xr[0] + xr[1]) + (xr[2] + xr[3]);
    s = x16only_sum(row_sum(s));
    const float mw = s * (1.f / 128.f);
    float d0 = xr[0] - mw, d1 = xr[1] - mw, d2 = xr[2] - mw, d3 = xr[3] - mw;
    float m2 = (d0 * d0 + d1 * d1) + (d2 * d2 + d3 * d3);
    m2 = x16only_sum(row_sum(m2));
    if (lane == 0) *(LAS v2f*)(L + L_ST + w * 8) = v2f{mw, m2};
    bar();
    const v4f a = *(const LAS v4f*)(L + L_ST), b = *(const LAS v4f*)(L + L_ST + 16);
    const float mean = ((a[0] + a[2]) + (b[0] + b[2])) * 0.25f;
    const float e0 = a[0] - mean, e1 = a[2] - mean, e2 = b[0] - mean, e3 = b[2] - mean;
    const float M2 = ((a[1] + a[3]) + (b[1] + b[3])) + 128.f * ((e0 * e0 + e1 * e1) + (e2 * e2 + e3 * e3));
    const float var = rbf(M2 * (1.f / 512.f));
    const float sd = rbf(__builtin_sqrtf(rbf(var + 1e-5f)));
    const float mb = rbf(mean);
    const float inv = 1.f / sd;
    v2u o;
    o.x = pk2(rbf(xr[0] - mb) * inv, rbf(xr[1] - mb) * inv);
    o.y = pk2(rbf(xr[2] - mb) * inv, rbf(xr[3] - mb) * inv);
    if (hf == 0) *(LAS v2u*)(L + L_ACT + xcol * 2) = o;
    LGKM0();
  };
  auto put_red = [&](const v4f (&acc)[8], int slot) {
    float v[8];
#pragma unroll
    for (int c = 0; c < 8; ++c) { const float t = kr ? acc[c][1] : acc[c][0]; v[c] = t + dpp<XOR8>(t); }
    if (lane < 8) {
      *(LAS v4f*)(L + L_RED + (slot * 64 + cb * 8) * 4) = v4f{v[0], v[1], v[2], v[3]};
      *(LAS v4f*)(L + L_RED + (slot * 64 + cb * 8 + 4) * 4) = v4f{v[4], v[5], v[6], v[7]};
    }
  };
  const v4f z4 = {0.f, 0.f, 0.f, 0.f};

#pragma unroll
  for (int l = 0; l < 2; ++l) {
    if (role == 0) {
      if (l == 0) { tr8(wr[0], wf[0]); tr8(wr[1], wf[1]); }
      else gather_x(WS_H5);
      if (w == 0) STAMP(l * 16 + 1);
      layernorm();
      v4f acc[8];
      const v4u a0 = *(const LAS v4u*)(L + L_ACT + (128 * w + 32 * (n16 & 1) + 8 * q4) * 2);
      const v4u a1 = *(const LAS v4u*)(L + L_ACT + (128 * w + 64 + 32 * (n16 & 1) + 8 * q4) * 2);
#pragma unroll
      for (int c = 0; c < 8; ++c) acc[c] = mfma(a1, wf[1][c], mfma(a0, wf[0][c], z4));
      put_red(acc, w);
      bar();
      if (w == 0) {
        const LAS float* rd = (const LAS float*)(L + L_RED);
        const float qv = (rd[lane] + rd[64 + lane]) + (rd[128 + lane] + rd[192 + lane]);
        const float qn = dpp<XOR1>(qv);
        if ((lane & 1) == 0) __builtin_amdgcn_raw_buffer_store_b64(v2u{pk2(qv, qn), tag}, rs, WS_H1 + l * 16384 + g * 2048 + ri * 256 + lane * 4, 0, AUX_SC1);
        STAMP(l * 16 + 2);
      }
    }
    if (w == 3) {
      if (D_H1 && (l == 1 || role == 0)) __builtin_amdgcn_s_sleep(D_H1);
      v4u v[2];
      for (u32 spins = 0; spins < SPIN_CAP; ++spins) {
#pragma unroll
        for (int k = 0; k < 2; ++k) v[k] = __builtin_amdgcn_raw_buffer_load_b128(rs, WS_H1 + l * 16384 + g * 2048 + k * 1024 + lane * 16, 0, AUX_SC1);
        const bool ok = (v[0].y == tag) & (v[0].w == tag) & (v[1].y == tag) & (v[1].w == tag);
        if (__all(ok)) break;
        __builtin_amdgcn_s_sleep(2);
      }
#pragma unroll
      for (int k = 0; k < 2; ++k) *(LAS v2u*)(L + L_QS + k * 512 + lane * 8) = v2u{v[k].x, v[k].z};
    }
    bar();
    if (l == 0) {
      if (w == 3) {
        v4u v[2];
        for (u32 spins = 0; spins < SPIN_CAP; ++spins) {
#pragma unroll
          for (int k = 0; k < 2; ++k) v[k] = __builtin_amdgcn_raw_buffer_load_b128(rs, WS_RDY + k * 1024 + lane * 16, 0, AUX_SC1);
          const bool ok = (v[0].x == tag) & (v[0].z == tag) & (v[1].x == tag) & (v[1].z == tag);
          if (__all(ok)) break;
          __builtin_amdgcn_s_sleep(2);
        }
        if (role != 0) issue_w();
      }
    }
    bar();
    if (l == 0 && role != 0) { tr8(wr[0], wf[0]); if (role != 3) tr8(wr[1], wf[1]); }
    if (role == 1) {
      if (D_H2) __builtin_amdgcn_s_sleep(D_H2);
      v4u pr[8]; float lse[8];
      const int hb = WS_H2 + l * H2_L + (g * 32 + 8 * q4) * H2_SLOT;
      for (u32 spins = 0; spins < SPIN_CAP; ++spins) {
        bool ok = true;
#pragma unroll
        for (int e = 0; e < 8; ++e) lse[e] = __uint_as_float(__builtin_amdgcn_raw_buffer_load_b32(rs, hb + e * H2_SLOT + 1024 + w * 4, 0, AUX_SC1));
#pragma unroll
        for (int e = 0; e < 8; ++e) pr[e] = __builtin_amdgcn_raw_buffer_load_b128(rs, hb + e * H2_SLOT + w * 256 + n16 * 16, 0, AUX_SC1);
#pragma unroll
        for (int e = 0; e < 8; ++e) ok &= nos4(pr[e]) & (__float_as_uint(lse[e]) != SENT);
        if (__all(ok)) break;
        __builtin_amdgcn_s_sleep(2);
      }
      if (w == 0) STAMP(l * 16 + 7);
      float M = fmaxf(fmaxf(fmaxf(lse[0], lse[1]), fmaxf(lse[2], lse[3])), fmaxf(fmaxf(lse[4], lse[5]), fmaxf(lse[6], lse[7])));
      M = x16_max(M);
      float Ls = 0.f;
#pragma unroll
      for (int e = 0; e < 8; ++e) { lse[e] = __builtin_amdgcn_exp2f(lse[e] - M); Ls += lse[e]; }
      Ls = x16_sum(Ls);
      const float inv = __builtin_amdgcn_rcpf(Ls);
#pragma unroll
      for (int e = 0; e < 8; ++e) { const float wv = lse[e] * inv, hi = rbf(wv); lse[e] = (n16 & 1) ? wv - hi : hi; }
      const v4u wa = {pk2(lse[0], lse[1]), pk2(lse[2], lse[3]), pk2(lse[4], lse[5]), pk2(lse[6], lse[7])};
      v4u pf[8];
      tr8(pr, pf);
      float ov[8];
#pragma unroll
      for (int c = 0; c < 8; ++c) { const v4f d = mfma(wa, pf[c], z4); ov[c] = d[0] + d[1]; }
      if (q4 == 0) *(LAS v4u*)(L + L_ACT + (128 * w + 8 * n16) * 2) = v4u{pk2(ov[0], ov[1]), pk2(ov[2], ov[3]), pk2(ov[4], ov[5]), pk2(ov[6], ov[7])};
      LGKM0();
      v4f acc[8];
      const v4u a0 = *(const LAS v4u*)(L + L_ACT + (128 * w + 32 * (n16 & 1) + 8 * q4) * 2);
      const v4u a1 = *(const LAS v4u*)(L + L_ACT + (128 * w + 64 + 32 * (n16 & 1) + 8 * q4) * 2);
#pragma unroll
      for (int c = 0; c < 8; ++c) acc[c] = mfma(a1, wf[1][c], mfma(a0, wf[0][c], z4));
      put_red(acc, w);
      bar();
      if (w == 0) {
        const LAS float* rd = (const LAS float*)(L + L_RED);
        const float y = (rd[lane] + rd[64 + lane]) + (rd[128 + lane] + rd[192 + lane]);
        __builtin_amdgcn_raw_buffer_store_b32(nosent(__float_as_uint(y)), rs, WS_H3 + l * 16384 + g * 2048 + ri * 256 + lane * 4, 0, AUX_SC1);
        STAMP(l * 16 + 8);
      }
    } else if (role == 2) {
      if (l == 1) gather_x(WS_H5);
      gather_x(WS_H3 + l * 16384);
      if (w == 0) STAMP(l * 16 + 9);
      layernorm();
      const int kh = ri & 1;
      const v4u a0 = *(const LAS v4u*)(L + L_ACT + (256 * kh + 64 * w + 32 * (n16 & 1) + 8 * q4) * 2);
      v4f acc[8];
#pragma unroll
      for (int c = 0; c < 8; ++c) acc[c] = mfma(a0, wf[0][c], z4);
      put_red(acc, w);
#pragma unroll
      for (int c = 0; c < 8; ++c) acc[c] = mfma(a0, wf[1][c], z4);
      put_red(acc, 4 + w);
      bar();
      if (w < 2 && wact) {
        const LAS float* rd = (const LAS float*)(L + L_RED) + w * 256;
        const float y = (rd[lane] + rd[64 + lane]) + (rd[128 + lane] + rd[192 + lane]);
        __builtin_amdgcn_raw_buffer_store_b32(nosent(__float_as_uint(y)), rs, WS_H4 + l * 32768 + g * 4096 + ri * 512 + w * 256 + lane * 4, 0, AUX_SC1);
      }
      if (w == 0) STAMP(l * 16 + 10);
    } else if (role == 3) {
      v4f acc[8];
      if (wact) {
        u32 v[4];
        const int hb = WS_H4 + l * 32768 + g * 4096 + w * 1024 + lane * 4;
        for (u32 spins = 0; spins < SPIN_CAP; ++spins) {
#pragma unroll
          for (int k = 0; k < 4; ++k) v[k] = __builtin_amdgcn_raw_buffer_load_b32(rs, hb + k * 256, 0, AUX_SC1);
          const bool ok = (v[0] != SENT) & (v[1] != SENT) & (v[2] != SENT) & (v[3] != SENT);
          if (__all(ok)) break;
          __builtin_amdgcn_s_sleep(2);
        }
        if (w == 0) STAMP(l * 16 + 11);
        const float gg = rbf(__uint_as_float(v[0]) + __uint_as_float(v[2])), uu = rbf(__uint_as_float(v[1]) + __uint_as_float(v[3]));
        const float sg = rbf(1.f / (1.f + __expf(-gg)));
        const float hv = sg * uu;
        *(LAS __bf16*)(L + L_ACT + (64 * w + lane) * 2) = (__bf16)hv;
        LGKM0();
        const v4u a0 = *(const LAS v4u*)(L + L_ACT + (64 * w + 32 * (n16 & 1) + 8 * q4) * 2);
#pragma unroll
        for (int c = 0; c < 8; ++c) acc[c] = mfma(a0, wf[0][c], z4);
      } else {
#pragma unroll
        for (int c = 0; c < 8; ++c) acc[c] = z4;
      }
      put_red(acc, w);
      bar();
      if (w == 0) {
        const LAS float* rd = (const LAS float*)(L + L_RED);
        const float y = (rd[lane] + rd[64 + lane]) + (rd[128 + lane] + rd[192 + lane]);
        __builtin_amdgcn_raw_buffer_store_b32(nosent(__float_as_uint(y)), rs, WS_H5 + l * 16384 + g * 2048 + ri * 256 + lane * 4, 0, AUX_SC1);
        STAMP(l * 16 + 12);
      }
    } else {
      if (l == 0 || cu == 0) gather_x(WS_H3 + l * 16384);
      if (l == 1 && cu == 0) {
        gather_x(WS_H5 + 16384);
        if (hf == 0) *(GAS v2u*)(p.out + xcol) = v2u{pk2(xr[0], xr[1]), pk2(xr[2], xr[3])};
        if (w == 0 && lane == 0) __builtin_amdgcn_raw_buffer_store_b32(tag, rs, WS_EPOCH, 0, AUX_SC1);
        if (w == 0) STAMP(l * 16 + 13);
        { const u64 t1 = __builtin_amdgcn_s_memrealtime(); const u64 tgt = t1 + ((t1 - t_entry) >> 20); while (__builtin_amdgcn_s_memrealtime() < tgt) __builtin_amdgcn_s_sleep(1); }
      }
    }
  }
}

extern "C" void kernel_launch(void* const* d_in, const int* in_sizes, int n_in, void* d_out, int out_size, void* d_ws, size_t ws_size,
                              hipStream_t stream) {
  Params p;
  p.x = (const u16*)d_in[0]; p.kc = (const u16*)d_in[1]; p.vc = (const u16*)d_in[2]; p.wq = (const u16*)d_in[3]; p.wo = (const u16*)d_in[4];
  p.wg = (const u16*)d_in[6]; p.wu = (const u16*)d_in[7]; p.wd = (const u16*)d_in[8]; p.out = (u16*)d_out; p.ws = (unsigned char*)d_ws;
  hipLaunchKernelGGL(fused_decode, dim3(256), dim3(512), 0, stream, p);
}
```

```cpp
#include <hip/hip_runtime.h>

typedef unsigned short u16;
typedef unsigned u32;
typedef unsigned long long u64;
typedef u32 v4u __attribute__((ext_vector_type(4)));
typedef u32 v2u __attribute__((ext_vector_type(2)));
typedef float v4f __attribute__((ext_vector_type(4)));
typedef float v2f __attribute__((ext_vector_type(2)));
typedef __bf16 bf16x8 __attribute__((ext_vector_type(8)));
typedef __bf16 bf16x2 __attribute__((ext_vector_type(2)));
typedef short s16x4 __attribute__((ext_vector_type(4)));
#define GAS __attribute__((address_space(1)))
#define LAS __attribute__((address_space(3)))

constexpr int SK = 32768, WIN = 4096, FF = 1792;
constexpr u32 SENT = 0xFFFFFFFFu;
constexpr int AUX_SC1 = 16;
constexpr u32 SPIN_CAP = 200000;

constexpr int WS_EPOCH = 0;
constexpr int WS_RDY = 4096;
constexpr int WS_CAN = 8192;
constexpr int WS_H1A = 16384;
constexpr int WS_H1B = WS_H1A + 131072;
constexpr int H2A_SLOT = 2176, H2B_SLOT = 1152;
constexpr int WS_H2A = WS_H1B + 65536;
constexpr int WS_H2B = WS_H2A + 256 * H2A_SLOT;
constexpr int WS_H3A = WS_H2B + 256 * H2B_SLOT;
constexpr int WS_H3B = WS_H3A + 32768;
constexpr int WS_H4A = WS_H3B + 16384;
constexpr int WS_H4B = WS_H4A + 65536;
constexpr int WS_H5A = WS_H4B + 32768;
constexpr int WS_H5B = WS_H5A + 32768;
constexpr int WS_END = WS_H5B + 16384;
constexpr int WS_STAMP = 2 << 20;
constexpr int WS_BYTES = 2 << 20;
static_assert(WS_END <= WS_BYTES, "ws map");

constexpr int L_QS = 0;
constexpr int L_PO = 1024;
constexpr int L_PM = 9216;
constexpr int L_ACT = 9472;
constexpr int L_ST = 10496;
constexpr int L_FLG = 10624;
constexpr int L_RED = 10752;
constexpr int LDS_BYTES = 96 * 1024;

#define KVWIN 16
#define KVW1 2
#define D_KV 1500
#define D_H10 1000
#define D_H1 900
#define D_H2 1500
#define D_H3 1200
#define D_H4 800
#define D_H5 600
#define D_H5B 700
#define D_H20 2500

struct Params {
  const u16* x; const u16* kc; const u16* vc; const u16* wq; const u16* wo;
  const u16* wg; const u16* wu; const u16* wd; u16* out; unsigned char* ws;
};

__device__ __forceinline__ float rbf(float x) { return (float)(__bf16)x; }
__device__ __forceinline__ u32 pk2(float lo, float hi) { bf16x2 v = {(__bf16)lo, (__bf16)hi}; return __builtin_bit_cast(u32, v); }
__device__ __forceinline__ float bflo(u32 w) { return __uint_as_float(w << 16); }
__device__ __forceinline__ float bfhi(u32 w) { return __uint_as_float(w & 0xffff0000u); }
__device__ __forceinline__ u32 nosent(u32 v) { return v == SENT ? 0x7FC07FC0u : v; }
__device__ __forceinline__ v4u ldnt(const void* p) { return __builtin_nontemporal_load((const GAS v4u*)p); }
__device__ __forceinline__ v4f mfma(v4u a, v4u b, v4f c) {
  return __builtin_amdgcn_mfma_f32_16x16x32_bf16(__builtin_bit_cast(bf16x8, a), __builtin_bit_cast(bf16x8, b), c, 0, 0, 0);
}
__device__ __forceinline__ v4f mfma16(v2u a, v2u b, v4f c) {
  return __builtin_amdgcn_mfma_f32_16x16x16bf16_1k(__builtin_bit_cast(s16x4, a), __builtin_bit_cast(s16x4, b), c, 0, 0, 0);
}
template <int CTRL> __device__ __forceinline__ float dpp(float x) {
  return __builtin_bit_cast(float, __builtin_amdgcn_mov_dpp(__builtin_bit_cast(int, x), CTRL, 0xf, 0xf, true));
}
template <int CTRL, int RM> __device__ __forceinline__ float dppm(float x) {
  return __builtin_bit_cast(float, __builtin_amdgcn_update_dpp(0, __builtin_bit_cast(int, x), CTRL, RM, 0xf, false));
}
constexpr int XOR1 = 0xB1, XOR2 = 0x4E, XOR7 = 0x141, XOR15 = 0x140, XOR8 = 0x128;
__device__ __forceinline__ float x16_max(float x) {
  auto s = __builtin_amdgcn_permlane16_swap(__float_as_uint(x), __float_as_uint(x), false, false);
  x = fmaxf(__uint_as_float(s[0]), __uint_as_float(s[1]));
  auto t = __builtin_amdgcn_permlane32_swap(__float_as_uint(x), __float_as_uint(x), false, false);
  return fmaxf(__uint_as_float(t[0]), __uint_as_float(t[1]));
}
__device__ __forceinline__ float wave_sum63(float x) {
  x += dpp<XOR1>(x); x += dpp<XOR2>(x); x += dpp<XOR7>(x); x += dpp<XOR15>(x);
  x += dppm<0x142, 0xa>(x);
  x += dppm<0x143, 0xc>(x);
  return x;
}
__device__ __forceinline__ float max8(const float (&s)[8]) {
  return fmaxf(fmaxf(fmaxf(s[0], s[1]), fmaxf(s[2], s[3])), fmaxf(fmaxf(s[4], s[5]), fmaxf(s[6], s[7])));
}
__device__ __forceinline__ u32 umax4(v4u v) { return max(max(v.x, v.y), max(v.z, v.w)); }
#define BAR() do { __builtin_amdgcn_sched_barrier(0); __syncthreads(); __builtin_amdgcn_sched_barrier(0); } while (0)
#define LGKM0() asm volatile("s_waitcnt lgkmcnt(0)" ::: "memory")
#define SB() __builtin_amdgcn_sched_barrier(0)
#define POLL _Pragma("nounroll") for (u32 spins = 0; spins < SPIN_CAP; ++spins)
#define KEEP8(a) asm volatile("" : "+v"(a[0]), "+v"(a[1]), "+v"(a[2]), "+v"(a[3]), "+v"(a[4]), "+v"(a[5]), "+v"(a[6]), "+v"(a[7]))
__device__ __forceinline__ u64 clk() { return __builtin_amdgcn_s_memtime(); }
__device__ __forceinline__ void wait_until(u64 t) { while (clk() < t) __builtin_amdgcn_s_sleep(1); }

#define STAMP(id) do { } while (0)
#define STAMP_INIT() do { } while (0)
#define STAMP_DUMP() do { } while (0)

__device__ __forceinline__ void tr8i(v4u* r) {
  v4u f[8];
#pragma unroll
  for (int c = 0; c < 8; ++c) {
    const u32 sel = (c & 1) ? 0x07060302u : 0x05040100u;
#pragma unroll
    for (int m = 0; m < 4; ++m) f[c][m] = __builtin_amdgcn_perm(r[2 * m + 1][c >> 1], r[2 * m][c >> 1], sel);
  }
#pragma unroll
  for (int c = 0; c < 8; ++c) r[c] = f[c];
}
__device__ __forceinline__ v2u tr4c(const v4u* r, int c) {
  const u32 sel = (c & 1) ? 0x07060302u : 0x05040100u;
  return v2u{__builtin_amdgcn_perm(r[1][c >> 1], r[0][c >> 1], sel), __builtin_amdgcn_perm(r[3][c >> 1], r[2][c >> 1], sel)};
}

template <int role, bool AW>
__device__ __forceinline__ void path(const Params& p, LAS unsigned char* L, const int v) {
  const int tid = threadIdx.x, lane = tid & 63, w = v & 3;
  const int cu = blockIdx.x, g = cu & 7, j = cu >> 3, ri = j & 7;
  const int q4 = lane >> 4, n16 = lane & 15, kr = n16 >> 3, cb = n16 & 7;
  const __amdgpu_buffer_rsrc_t rs = __builtin_amdgcn_make_buffer_rsrc(p.ws, 0, WS_BYTES, 0x00020000);
  constexpr float LOG2E = 1.4426950408889634f;
  const v4f z4 = {0.f, 0.f, 0.f, 0.f};
  const int nblk = g < 4 ? 4 : 3;
  const int rng = (n16 >> 2) & 1;
  volatile LAS u32* flg = (volatile LAS u32*)(L + L_FLG);

  v4u ra[AW ? 16 : 4];
  v4u xm; v2u xa; u32 ep, xraw;
  if constexpr (AW) {
    ep = __builtin_amdgcn_raw_buffer_load_b32(rs, WS_EPOCH, 0, AUX_SC1);
    xraw = *(const GAS u16*)(p.x + tid);
    const size_t kb = ((size_t)g * SK + (SK - WIN) + j * 128 + w * 32) * 128;
    wait_until(clk() + D_KV);
#pragma unroll
    for (int i = 0; i < 16; ++i) {
      if (i >= KVW1) { SB(); if (flg[4] == 0u) asm volatile("s_waitcnt vmcnt(%0)" :: "i"(KVW1 - 1) : "memory"); SB(); }
      if (i >= KVWIN) { SB(); asm volatile("s_waitcnt vmcnt(%0)" :: "i"(KVWIN - 1) : "memory"); SB(); }
      if (i < 8) ra[i] = ldnt(p.kc + kb + ((i >> 2) * 16 + n16) * 128 + (i & 3) * 32 + q4 * 8);
      else { const int e = i - 8; ra[i] = ldnt(p.vc + kb + (16 * (e >> 2) + 4 * q4 + (e & 3)) * 128 + n16 * 8); }
    }
  } else {
    const u16* src = p.wq + (size_t)(128 * role + 32 * w + 16 * kr + 4 * q4) * 4096 + g * 512 + ri * 64 + cb * 8;
#pragma unroll
    for (int e = 0; e < 4; ++e) ra[e] = ldnt(src + (size_t)e * 4096);
    xm = *(const GAS v4u*)(p.x + lane * 8);
    xa = *(const GAS v2u*)(p.x + 128 * role + 32 * w + 16 * rng + 4 * q4);
    ep = __builtin_amdgcn_raw_buffer_load_b32(rs, WS_EPOCH, 0, AUX_SC1);
    xraw = *(const GAS u16*)(p.x + tid);
    SB();
  }
  SB();
  if (AW && w == 0) STAMP(15);

  constexpr bool HASW = role == 1 || role == 2 || (role == 3 && !AW);
  v4u wr[8];
  bool wact = true;
  auto issue_w = [&]() {
    const int rl = 32 * kr + 8 * q4;
    const u16* wsrc; int pitch = 512;
    if (role == 2) {
      const int bi = ri >> 1; const int blk = bi < nblk ? g + 8 * bi : g;
      pitch = FF; wsrc = (AW ? p.wg : p.wu) + (size_t)(256 * (ri & 1) + 64 * w + rl) * FF + blk * 64 + cb * 8;
    } else if (role == 3) {
      const int blk = w < nblk ? g + 8 * w : g;
      wsrc = p.wd + (size_t)(blk * 64 + rl) * 512 + ri * 64 + cb * 8;
    } else wsrc = p.wo + (size_t)(g * 512 + 64 * v + rl) * 512 + ri * 64 + cb * 8;
    SB();
#pragma unroll
    for (int e = 0; e < 8; ++e) wr[e] = ldnt(wsrc + (size_t)e * pitch);
    SB();
  };
  if (role == 2) wact = (ri >> 1) < nblk;
  if (role == 3) wact = w < nblk;

  float xr;
  u32 tag;
  auto gather_x = [&](int base) {
    float s;
    POLL {
      u32 t[8];
#pragma unroll
      for (int k = 0; k < 8; ++k) t[k] = __builtin_amdgcn_raw_buffer_load_b32(rs, base + k * 2048 + tid * 4, 0, AUX_SC1);
      s = ((__uint_as_float(t[0]) + __uint_as_float(t[1])) + (__uint_as_float(t[2]) + __uint_as_float(t[3]))) +
          ((__uint_as_float(t[4]) + __uint_as_float(t[5])) + (__uint_as_float(t[6]) + __uint_as_float(t[7])));
      if (!__any(s != s)) break;
      const u32 mx = max(max(max(t[0], t[1]), max(t[2], t[3])), max(max(t[4], t[5]), max(t[6], t[7])));
      if (!__any(mx == SENT)) break;
      __builtin_amdgcn_s_sleep(2);
    }
    xr = rbf(xr + rbf(s));
  };
  auto gather_x_tagged = [&](int base) {
    float s;
    POLL {
      v2u t[8];
#pragma unroll
      for (int k = 0; k < 8; ++k) t[k] = __builtin_amdgcn_raw_buffer_load_b64(rs, base + k * 4096 + tid * 8, 0, AUX_SC1);
      s = ((__uint_as_float(t[0].x) + __uint_as_float(t[1].x)) + (__uint_as_float(t[2].x) + __uint_as_float(t[3].x))) +
          ((__uint_as_float(t[4].x) + __uint_as_float(t[5].x)) + (__uint_as_float(t[6].x) + __uint_as_float(t[7].x)));
      const u32 bad = (((t[0].y ^ tag) | (t[1].y ^ tag)) | ((t[2].y ^ tag) | (t[3].y ^ tag))) | (((t[4].y ^ tag) | (t[5].y ^ tag)) | ((t[6].y ^ tag) | (t[7].y ^ tag)));
      if (!__any(bad != 0u)) break;
      __builtin_amdgcn_s_sleep(2);
    }
    xr = rbf(xr + rbf(s));
  };
  auto layernorm = [&]() {
    const float s1 = wave_sum63(xr), s2 = wave_sum63(xr * xr);
    if (lane == 63) *(LAS v2f*)(L + L_ST + v * 8) = v2f{s1, s2};
    BAR();
    const v4f a = *(const LAS v4f*)(L + L_ST), b = *(const LAS v4f*)(L + L_ST + 16), c = *(const LAS v4f*)(L + L_ST + 32), d = *(const LAS v4f*)(L + L_ST + 48);
    const v4f t = (a + b) + (c + d);
    const float mean = (t[0] + t[2]) * (1.f / 512.f), ex2 = (t[1] + t[3]) * (1.f / 512.f);
    const float var = rbf(fmaxf(__builtin_fmaf(-mean, mean, ex2), 0.f));
    const float inv = __builtin_amdgcn_rcpf(rbf(__builtin_amdgcn_sqrtf(rbf(var + 1e-5f))));
    *(LAS __bf16*)(L + L_ACT + tid * 2) = (__bf16)(rbf(xr - rbf(mean)) * inv);
    BAR();
  };
  const bool useful = q4 < 2 && q4 == kr;
  auto put_red = [&](v4f (&acc)[8], int slot) {
    KEEP8(acc);
    if (useful) {
      LAS float* dst = (LAS float*)(L + L_RED) + (slot * 2 + kr) * 64 + cb * 8;
#pragma unroll
      for (int c = 0; c < 8; ++c) dst[c] = acc[c][0];
    }
  };
  auto gemv_tile = [&](int blk, int slot) {
    const v4u a = *(const LAS v4u*)(L + L_ACT + (64 * blk + 32 * rng + 8 * q4) * 2);
    v4f acc[8];
#pragma unroll
    for (int c = 0; c < 8; ++c) acc[c] = mfma(a, wr[c], z4);
    put_red(acc, slot);
  };
  const LAS float* rd = (const LAS float*)(L + L_RED) + lane;
  auto sum8 = [&](const LAS float* r) {
    float t[8];
#pragma unroll
    for (int k = 0; k < 8; ++k) t[k] = r[64 * k];
    return ((t[0] + t[1]) + (t[2] + t[3])) + ((t[4] + t[5]) + (t[6] + t[7]));
  };
  auto hint_put = [&](int idx) {
    if (ri == 0 && v == 4 && lane == 0) __builtin_amdgcn_raw_buffer_store_b32(tag, rs, WS_CAN + (idx * 8 + g) * 128, 0, AUX_SC1);
  };
  auto hint_wait = [&](int idx, int delta) {
    if (v == 0) {
      POLL {
        if (__builtin_amdgcn_readfirstlane(__builtin_amdgcn_raw_buffer_load_b32(rs, WS_CAN + (idx * 8 + g) * 128, 0, AUX_SC1)) == tag) break;
        __builtin_amdgcn_s_sleep(2);
      }
    }
    BAR();
    wait_until(clk() + delta);
  };
  auto reset_slots = [&]() {
    if (v == 7) {
      const v4u sv = {SENT, SENT, SENT, SENT};
      const int slot = WS_H2B + (g * 32 + j) * H2B_SLOT;
      __builtin_amdgcn_raw_buffer_store_b128(sv, rs, slot + lane * 16, 0, AUX_SC1);
      int off = WS_BYTES;
      if (lane < 16) off = WS_H1B + g * 8192 + role * 2048 + ri * 256 + lane * 16;
      if (lane == 16) off = slot + 1024;
      if (lane >= 32) {
        const int t = lane - 32;
        if (role == 1 && t < 16) off = WS_H3B + g * 2048 + ri * 256 + t * 16;
        if (role == 2) off = WS_H4B + g * 4096 + ri * 512 + t * 16;
        if (role == 3 && t < 16) off = WS_H5B + g * 2048 + ri * 256 + t * 16;
      }
      __builtin_amdgcn_raw_buffer_store_b128(sv, rs, off, 0, AUX_SC1);
      asm volatile("s_waitcnt vmcnt(0)" ::: "memory");
      if (lane == 0) __builtin_amdgcn_raw_buffer_store_b64(v2u{tag, tag}, rs, WS_RDY + (g * 32 + j) * 8, 0, AUX_SC1);
    }
  };
  v2u qw[8];
  u64 tp = 0, tq = 0;

#pragma unroll
  for (int l = 0; l < 2; ++l) {
    if (l == 0) {
      if constexpr (!AW) {
        float s1 = 0.f, s2 = 0.f;
#pragma unroll
        for (int k = 0; k < 4; ++k) {
          const float a0 = bflo(xm[k]), a1 = bfhi(xm[k]);
          s1 += a0 + a1; s2 = __builtin_fmaf(a0, a0, __builtin_fmaf(a1, a1, s2));
        }
        const float mean = __builtin_bit_cast(float, __builtin_amdgcn_readlane(__builtin_bit_cast(int, wave_sum63(s1)), 63)) * (1.f / 512.f);
        const float ex2 = __builtin_bit_cast(float, __builtin_amdgcn_readlane(__builtin_bit_cast(int, wave_sum63(s2)), 63)) * (1.f / 512.f);
        const float var = rbf(fmaxf(__builtin_fmaf(-mean, mean, ex2), 0.f));
        const float inv = __builtin_amdgcn_rcpf(rbf(__builtin_amdgcn_sqrtf(rbf(var + 1e-5f))));
        const float mb = rbf(mean);
        v2u a;
#pragma unroll
        for (int k = 0; k < 2; ++k) { const u32 d = pk2(bflo(xa[k]) - mb, bfhi(xa[k]) - mb); a[k] = pk2(bflo(d) * inv, bfhi(d) * inv); }
        if (w == 0) STAMP(1);
        v4f acc[8];
#pragma unroll
        for (int c = 0; c < 8; ++c) { qw[c] = tr4c(ra, c); acc[c] = mfma16(a, qw[c], z4); }
        put_red(acc, w);
        LGKM0();
        tag = __builtin_amdgcn_readfirstlane(ep) + 1u;
        flg[w] = 1u;
        if (w == 0) {
          POLL { const v4u f = *(volatile LAS v4u*)(L + L_FLG); if ((f.x & f.y & f.z & f.w) != 0u) break; }
          __builtin_amdgcn_raw_buffer_store_b64(v2u{__float_as_uint(sum8(rd)), tag}, rs, WS_H1A + g * 16384 + role * 4096 + ri * 512 + lane * 8, 0, AUX_SC1);
          flg[4] = 1u;
          STAMP(2);
        } else { POLL { if (flg[4] != 0u) break; __builtin_amdgcn_s_sleep(1); } }
        tq = clk();
        xr = bflo(xraw);
      }
    } else {
      if (role < 2) { hint_wait(1, 0); gather_x_tagged(WS_H3A); }
      if (role != 3) hint_wait(2, D_H5); else wait_until(clk() + D_H5B);
      gather_x_tagged(WS_H5A);
      if (v == 4) STAMP(17);
      layernorm();
      if constexpr (!AW) {
        const v2u a = *(const LAS v2u*)(L + L_ACT + (128 * role + 32 * w + 16 * rng + 4 * q4) * 2);
        v4f acc[8];
#pragma unroll
        for (int c = 0; c < 8; ++c) acc[c] = mfma16(a, qw[c], z4);
        put_red(acc, w);
      }
      BAR();
      if (v == 4) {
        __builtin_amdgcn_raw_buffer_store_b32(nosent(__float_as_uint(sum8(rd))), rs, WS_H1B + g * 8192 + role * 2048 + ri * 256 + lane * 4, 0, AUX_SC1);
        STAMP(18);
      }
    }
    if constexpr (!AW) {
      if (l == 1) tq = clk();
      wait_until(tq + (l == 0 ? D_H10 : D_H1));
      v2f qs;
      if (l == 0) {
        POLL {
          v4u t[4];
#pragma unroll
          for (int k = 0; k < 4; ++k) t[k] = __builtin_amdgcn_raw_buffer_load_b128(rs, WS_H1A + g * 16384 + k * 4096 + w * 1024 + lane * 16, 0, AUX_SC1);
          qs[0] = (__uint_as_float(t[0].x) + __uint_as_float(t[1].x)) + (__uint_as_float(t[2].x) + __uint_as_float(t[3].x));
          qs[1] = (__uint_as_float(t[0].z) + __uint_as_float(t[1].z)) + (__uint_as_float(t[2].z) + __uint_as_float(t[3].z));
          const u32 bad = ((t[0].y ^ tag) | (t[0].w ^ tag)) | ((t[1].y ^ tag) | (t[1].w ^ tag)) | ((t[2].y ^ tag) | (t[2].w ^ tag)) | ((t[3].y ^ tag) | (t[3].w ^ tag));
          if (!__any(bad != 0u)) break;
          __builtin_amdgcn_s_sleep(4);
        }
      } else {
        POLL {
          v2u t[4];
#pragma unroll
          for (int k = 0; k < 4; ++k) t[k] = __builtin_amdgcn_raw_buffer_load_b64(rs, WS_H1B + g * 8192 + k * 2048 + w * 512 + lane * 8, 0, AUX_SC1);
          qs = (__builtin_bit_cast(v2f, t[0]) + __builtin_bit_cast(v2f, t[1])) + (__builtin_bit_cast(v2f, t[2]) + __builtin_bit_cast(v2f, t[3]));
          const float c = qs[0] + qs[1];
          if (!__any(c != c)) break;
          const u32 mx = max(max(max(t[0].x, t[0].y), max(t[1].x, t[1].y)), max(max(t[2].x, t[2].y), max(t[3].x, t[3].y)));
          if (!__any(mx == SENT)) break;
          __builtin_amdgcn_s_sleep(2);
        }
      }
      *(LAS u32*)(L + L_QS + w * 256 + lane * 4) = pk2(qs[0], qs[1]);
      if (w == 0 && l == 0) STAMP(14);
    }
    BAR();
    if constexpr (AW) {
      if (w == 0) STAMP(l * 16 + 3);
      if (l == 0) { tag = __builtin_amdgcn_readfirstlane(ep) + 1u; xr = bflo(xraw); }
      v4u qf[4];
#pragma unroll
      for (int s = 0; s < 4; ++s) qf[s] = *(const LAS v4u*)(L + L_QS + ((n16 >> 2) * 128 + s * 32 + q4 * 8) * 2);
      v4f sc[2];
#pragma unroll
      for (int t = 0; t < 2; ++t) {
        sc[t] = z4;
#pragma unroll
        for (int s = 0; s < 4; ++s) sc[t] = mfma(ra[t * 4 + s], qf[s], sc[t]);
      }
      float s8[8];
#pragma unroll
      for (int e = 0; e < 8; e += 2) {
        const u32 r1 = pk2(sc[e >> 2][e & 3], sc[e >> 2][(e & 3) + 1]);
        const u32 r2 = pk2(bflo(r1) * 0.08838834764831845f, bfhi(r1) * 0.08838834764831845f);
        s8[e] = bflo(r2); s8[e + 1] = bfhi(r2);
      }
      const float m = x16_max(max8(s8)) * LOG2E;
#pragma unroll
      for (int e = 0; e < 8; ++e) s8[e] = __builtin_amdgcn_exp2f(__builtin_fmaf(s8[e], LOG2E, -m));
      const v4u pa = {pk2(s8[0], s8[1]), pk2(s8[2], s8[3]), pk2(s8[4], s8[5]), pk2(s8[6], s8[7])};
      if (w == 0) STAMP(l * 16 + 4);
      if (l == 0) { if (role == 1) issue_w(); tr8i(ra + 8); }
      v4f o[8];
#pragma unroll
      for (int c = 0; c < 8; ++c) o[c] = mfma(pa, ra[8 + c], z4);
      const v4f ls = mfma(pa, v4u{0x3F803F80u, 0x3F803F80u, 0x3F803F80u, 0x3F803F80u}, z4);
      KEEP8(o);
      LAS float* dst = (LAS float*)(L + L_PO) + (w * 4 + q4) * 128 + n16 * 8;
#pragma unroll
      for (int c = 0; c < 8; ++c) dst[c] = o[c][0];
      if (n16 == 4 * q4) *(LAS v2f*)(L + L_PM + (q4 * 4 + w) * 8) = v2f{m, ls[0]};
    } else if (l == 0 && role == 1) issue_w();
    BAR();
    tp = clk();
    if constexpr (AW) {
      if (w == 0) STAMP(l * 16 + 5);
      const v4f ml0 = *(const LAS v4f*)(L + L_PM + w * 32), ml1 = *(const LAS v4f*)(L + L_PM + w * 32 + 16);
      v2f pv[4];
#pragma unroll
      for (int u = 0; u < 4; ++u) pv[u] = *(const LAS v2f*)(L + L_PO + ((u * 4 + w) * 128 + lane * 2) * 4);
      const float M = fmaxf(fmaxf(ml0[0], ml0[2]), fmaxf(ml1[0], ml1[2]));
      const float s0 = __builtin_amdgcn_exp2f(ml0[0] - M), s1 = __builtin_amdgcn_exp2f(ml0[2] - M);
      const float s2 = __builtin_amdgcn_exp2f(ml1[0] - M), s3 = __builtin_amdgcn_exp2f(ml1[2] - M);
      const float Ls = (s0 * ml0[1] + s1 * ml0[3]) + (s2 * ml1[1] + s3 * ml1[3]);
      const float inv = __builtin_amdgcn_rcpf(Ls);
      const v2f ov = ((s0 * pv[0] + s1 * pv[1]) + (s2 * pv[2] + s3 * pv[3])) * inv;
      const u32 lse = __float_as_uint(M + __builtin_amdgcn_logf(Ls));
      if (l == 0) {
        const int slot = WS_H2A + (g * 32 + j) * H2A_SLOT;
        __builtin_amdgcn_raw_buffer_store_b64(v2u{pk2(ov[0], ov[1]), tag}, rs, slot + w * 512 + lane * 8, 0, AUX_SC1);
        if (lane == 0) __builtin_amdgcn_raw_buffer_store_b64(v2u{lse, tag}, rs, slot + 2048 + w * 8, 0, AUX_SC1);
      } else {
        const int slot = WS_H2B + (g * 32 + j) * H2B_SLOT;
        __builtin_amdgcn_raw_buffer_store_b32(nosent(pk2(ov[0], ov[1])), rs, slot + w * 256 + lane * 4, 0, AUX_SC1);
        if (lane == 0) __builtin_amdgcn_raw_buffer_store_b32(nosent(lse), rs, slot + 1024 + w * 4, 0, AUX_SC1);
      }
      if (w == 0) STAMP(l * 16 + 6);
    }
    if (l == 0 && role != 1) reset_slots();
    if (l == 0 && HASW) { if (role != 1) issue_w(); tr8i(wr); }
    if constexpr (role == 1) {
      wait_until(tp + (l == 0 ? D_H20 : D_H2));
      v4u pr[4]; float u[4];
      if (l == 0) {
        const int hb = WS_H2A + (g * 32 + 4 * q4) * H2A_SLOT;
        const int po = hb + 16 * kr * H2A_SLOT + v * 256 + cb * 32;
        const int lo = hb + 16 * (n16 & 1) * H2A_SLOT + 2048 + (v >> 1) * 8;
        POLL {
          v2u lg[4]; v4u a[4], b[4];
#pragma unroll
          for (int e = 0; e < 4; ++e) lg[e] = __builtin_amdgcn_raw_buffer_load_b64(rs, lo + e * H2A_SLOT, 0, AUX_SC1);
#pragma unroll
          for (int e = 0; e < 4; ++e) {
            a[e] = __builtin_amdgcn_raw_buffer_load_b128(rs, po + e * H2A_SLOT, 0, AUX_SC1);
            b[e] = __builtin_amdgcn_raw_buffer_load_b128(rs, po + e * H2A_SLOT + 16, 0, AUX_SC1);
          }
          u32 bad = 0;
#pragma unroll
          for (int e = 0; e < 4; ++e) {
            bad |= ((a[e].y ^ tag) | (a[e].w ^ tag)) | ((b[e].y ^ tag) | (b[e].w ^ tag)) | (lg[e].y ^ tag);
            pr[e] = v4u{a[e].x, a[e].z, b[e].x, b[e].z}; u[e] = __uint_as_float(lg[e].x);
          }
          if (!__any(bad != 0u)) break;
          __builtin_amdgcn_s_sleep(2);
        }
      } else {
        const int hb = WS_H2B + (g * 32 + 4 * q4) * H2B_SLOT;
        const int po = hb + 16 * kr * H2B_SLOT + v * 128 + cb * 16;
        const int lo = hb + 16 * (n16 & 1) * H2B_SLOT + 1024 + (v >> 1) * 4;
        POLL {
#pragma unroll
          for (int e = 0; e < 4; ++e) u[e] = __uint_as_float(__builtin_amdgcn_raw_buffer_load_b32(rs, lo + e * H2B_SLOT, 0, AUX_SC1));
#pragma unroll
          for (int e = 0; e < 4; ++e) pr[e] = __builtin_amdgcn_raw_buffer_load_b128(rs, po + e * H2B_SLOT, 0, AUX_SC1);
          const u32 mx = max(max(max(umax4(pr[0]), umax4(pr[1])), max(umax4(pr[2]), umax4(pr[3]))),
                             max(max(__float_as_uint(u[0]), __float_as_uint(u[1])), max(__float_as_uint(u[2]), __float_as_uint(u[3]))));
          if (!__any(mx == SENT)) break;
          __builtin_amdgcn_s_sleep(2);
        }
      }
      hint_put(l * 3 + 0);
      if (v == 4) STAMP(l * 16 + 7);
      float M = fmaxf(fmaxf(u[0], u[1]), fmaxf(u[2], u[3]));
      M = fmaxf(M, dpp<XOR1>(M));
      M = x16_max(M);
#pragma unroll
      for (int e = 0; e < 4; ++e) u[e] = __builtin_amdgcn_exp2f(u[e] - M);
      const v2u wa = {pk2(u[0], u[1]), pk2(u[2], u[3])};
      const v4f dn = mfma16(wa, v2u{0x3F803F80u, 0x3F803F80u}, z4);
      v4f d[8];
#pragma unroll
      for (int c = 0; c < 8; ++c) d[c] = mfma16(wa, tr4c(pr, c), z4);
      KEEP8(d);
      const float inv = __builtin_amdgcn_rcpf(dn[0] + dn[1]);
      float ov[8];
#pragma unroll
      for (int c = 0; c < 8; ++c) { const float t = kr ? d[c][1] : d[c][0]; ov[c] = (t + dpp<XOR8>(t)) * inv; }
      if (lane < 8) *(LAS v4u*)(L + L_ACT + (64 * v + 8 * cb) * 2) = v4u{pk2(ov[0], ov[1]), pk2(ov[2], ov[3]), pk2(ov[4], ov[5]), pk2(ov[6], ov[7])};
      LGKM0();
      gemv_tile(v, v);
      BAR();
      if (v == 4) {
        const float y = sum8(rd) + sum8(rd + 512);
        if (l == 0) __builtin_amdgcn_raw_buffer_store_b64(v2u{__float_as_uint(y), tag}, rs, WS_H3A + g * 4096 + ri * 512 + lane * 8, 0, AUX_SC1);
        else __builtin_amdgcn_raw_buffer_store_b32(nosent(__float_as_uint(y)), rs, WS_H3B + g * 2048 + ri * 256 + lane * 4, 0, AUX_SC1);
        STAMP(l * 16 + 8);
      }
      if (l == 0) reset_slots();
    } else if constexpr (role == 2) {
      hint_wait(l * 3 + 0, D_H3);
      if (l == 0) gather_x_tagged(WS_H3A); else gather_x(WS_H3B);
      hint_put(l * 3 + 1);
      if (v == 4) STAMP(l * 16 + 9);
      layernorm();
      gemv_tile(4 * (ri & 1) + w, v);
      BAR();
      if (!AW && w < 2 && wact) {
        const u32 y = __float_as_uint(sum8(rd + w * 512));
        if (l == 0) __builtin_amdgcn_raw_buffer_store_b64(v2u{y, tag}, rs, WS_H4A + g * 8192 + ri * 1024 + w * 512 + lane * 8, 0, AUX_SC1);
        else __builtin_amdgcn_raw_buffer_store_b32(nosent(y), rs, WS_H4B + g * 4096 + ri * 512 + w * 256 + lane * 4, 0, AUX_SC1);
      }
      if (v == 4) STAMP(l * 16 + 10);
    } else if constexpr (role == 3) {
      hint_wait(l * 3 + 1, 0);
      const u64 th = clk();
      if (l == 0) gather_x_tagged(WS_H3A);
      if (l == 0 && v == 0) {
        POLL {
          const v2u t = __builtin_amdgcn_raw_buffer_load_b64(rs, WS_RDY + g * 256 + (lane & 31) * 8, 0, AUX_SC1);
          if (!__any(t.x != tag)) break;
          __builtin_amdgcn_s_sleep(4);
        }
      }
      if constexpr (!AW) {
        float hv = 0.f;
        if (wact) {
          wait_until(th + D_H4);
          float gg, uu;
          if (l == 0) {
            const int hb = WS_H4A + g * 8192 + w * 2048 + lane * 8;
            POLL {
              v2u t[4];
#pragma unroll
              for (int k = 0; k < 4; ++k) t[k] = __builtin_amdgcn_raw_buffer_load_b64(rs, hb + k * 512, 0, AUX_SC1);
              gg = __uint_as_float(t[0].x) + __uint_as_float(t[2].x); uu = __uint_as_float(t[1].x) + __uint_as_float(t[3].x);
              const u32 bad = ((t[0].y ^ tag) | (t[1].y ^ tag)) | ((t[2].y ^ tag) | (t[3].y ^ tag));
              if (!__any(bad != 0u)) break;
              __builtin_amdgcn_s_sleep(2);
            }
          } else {
            const int hb = WS_H4B + g * 4096 + w * 1024 + lane * 4;
            POLL {
              u32 t[4];
#pragma unroll
              for (int k = 0; k < 4; ++k) t[k] = __builtin_amdgcn_raw_buffer_load_b32(rs, hb + k * 256, 0, AUX_SC1);
              gg = __uint_as_float(t[0]) + __uint_as_float(t[2]); uu = __uint_as_float(t[1]) + __uint_as_float(t[3]);
              const u32 mx = max(max(t[0], t[1]), max(t[2], t[3]));
              if (!__any(mx == SENT)) break;
              __builtin_amdgcn_s_sleep(2);
            }
          }
          hint_put(l * 3 + 2);
          if (w == 0) STAMP(l * 16 + 11);
          const u32 gu = pk2(gg, uu);
          hv = rbf(__builtin_amdgcn_rcpf(1.f + __builtin_amdgcn_exp2f(-LOG2E * bflo(gu)))) * bfhi(gu);
        }
        *(LAS __bf16*)(L + L_ACT + tid * 2) = (__bf16)hv;
        LGKM0();
        gemv_tile(v, w);
      }
      BAR();
      if (v == 4) {
        const u32 y = __float_as_uint(sum8(rd));
        if (l == 0) __builtin_amdgcn_raw_buffer_store_b64(v2u{y, tag}, rs, WS_H5A + g * 4096 + ri * 512 + lane * 8, 0, AUX_SC1);
        else __builtin_amdgcn_raw_buffer_store_b32(nosent(y), rs, WS_H5B + g * 2048 + ri * 256 + lane * 4, 0, AUX_SC1);
        STAMP(l * 16 + 12);
      }
    } else {
      if (l == 1 && j == 0) {
        hint_wait(4, 0);
        if (v == g) gather_x(WS_H3B);
        hint_wait(5, D_H5);
        if (v == g) {
          gather_x(WS_H5B);
          *(GAS __bf16*)(p.out + tid) = (__bf16)xr;
          STAMP(29);
        }
      }
      if (l == 1 && cu == 0 && v == 4 && lane == 0) __builtin_amdgcn_raw_buffer_store_b32(tag, rs, WS_EPOCH, 0, AUX_SC1);
    }
  }
}

__global__ void __launch_bounds__(512, 2) fused_decode(Params p) {
  __shared__ __attribute__((aligned(16))) unsigned char lds_raw[LDS_BYTES];
  LAS unsigned char* L = (LAS unsigned char*)lds_raw;
  const int v = __builtin_amdgcn_readfirstlane(threadIdx.x >> 6);
  const int role = blockIdx.x >> 6;
  if (threadIdx.x < 8) *(LAS u32*)(L + L_FLG + threadIdx.x * 4) = 0u;
  STAMP_INIT();
  __syncthreads();
  asm volatile("" :: "s"(p.x), "s"(p.kc), "s"(p.vc), "s"(p.wq), "s"(p.wo), "s"(p.wg), "s"(p.wu), "s"(p.wd), "s"(p.out), "s"(p.ws));
  if (v < 4) {
    if (role == 0) path<0, true>(p, L, v);
    else if (role == 1) path<1, true>(p, L, v);
    else if (role == 2) path<2, true>(p, L, v);
    else path<3, true>(p, L, v);
  } else {
    if (role == 0) path<0, false>(p, L, v);
    else if (role == 1) path<1, false>(p, L, v);
    else if (role == 2) path<2, false>(p, L, v);
    else path<3, false>(p, L, v);
  }
  STAMP_DUMP();
}

extern "C" void kernel_launch(void* const* d_in, const int* in_sizes, int n_in, void* d_out, int out_size, void* d_ws, size_t ws_size,
                              hipStream_t stream) {
  Params p;
  p.x = (const u16*)d_in[0]; p.kc = (const u16*)d_in[1]; p.vc = (const u16*)d_in[2]; p.wq = (const u16*)d_in[3]; p.wo = (const u16*)d_in[4];
  p.wg = (const u16*)d_in[6]; p.wu = (const u16*)d_in[7]; p.wd = (const u16*)d_in[8]; p.out = (u16*)d_out; p.ws = (unsigned char*)d_ws;
  hipLaunchKernelGGL(fused_decode, dim3(256), dim3(512), 0, stream, p);
}
```

```cpp
#include <hip/hip_runtime.h>

typedef unsigned short u16;
typedef unsigned u32;
typedef unsigned long long u64;
typedef u32 v4u __attribute__((ext_vector_type(4)));
typedef u32 v2u __attribute__((ext_vector_type(2)));
typedef float v4f __attribute__((ext_vector_type(4)));
typedef float v2f __attribute__((ext_vector_type(2)));
typedef __bf16 bf16x8 __attribute__((ext_vector_type(8)));
typedef __bf16 bf16x2 __attribute__((ext_vector_type(2)));
typedef short s16x4 __attribute__((ext_vector_type(4)));
#define GAS __attribute__((address_space(1)))
#define LAS __attribute__((address_space(3)))

constexpr int SK = 32768, WIN = 4096, FF = 1792;
constexpr u32 SENT = 0xFFFFFFFFu;
constexpr int AUX_SC1 = 16;
constexpr u32 SPIN_CAP = 200000;

constexpr int WS_EPOCH = 0;
constexpr int WS_RDY = 4096;
constexpr int WS_CAN = 8192;
constexpr int WS_H1A = 16384;
constexpr int WS_H1B = WS_H1A + 131072;
constexpr int H2_SLOT = 1152, H2_L = 256 * H2_SLOT;
constexpr int WS_H2 = WS_H1B + 65536;
constexpr int WS_H3 = WS_H2 + 2 * H2_L;
constexpr int WS_H4 = WS_H3 + 32768;
constexpr int WS_H5 = WS_H4 + 65536;
constexpr int WS_END = WS_H5 + 32768;
constexpr int WS_STAMP = 2 << 20;
constexpr int WS_BYTES = 1 << 20;
static_assert(WS_END <= WS_BYTES, "ws map");

constexpr int L_QS = 0;
constexpr int L_PO = 1024;
constexpr int L_PM = 9216;
constexpr int L_ACT = 9472;
constexpr int L_ST = 10496;
constexpr int L_FLG = 10624;
constexpr int L_RED = 10752;
constexpr int L_W = 20480;
constexpr int LDS_BYTES = L_W + 65536;

#define CODE_BYTES 0
#define KVREL 5
#define DMAW 16
#define KVW1 4
#define D_KV 0
#define D_H10 1400
#define D_H1 700
#define D_H2 700
#define D_H3 1200
#define D_H4 800
#define D_H5 600
#define D_H5B 700
#define D_H20 1200
#define D_W 2500
#define D_W2 8000

struct Params {
  const u16* x; const u16* kc; const u16* vc; const u16* wq; const u16* wo;
  const u16* wg; const u16* wu; const u16* wd; u16* out; unsigned char* ws;
};

__device__ __forceinline__ float rbf(float x) { return (float)(__bf16)x; }
__device__ __forceinline__ u32 pk2(float lo, float hi) { bf16x2 v = {(__bf16)lo, (__bf16)hi}; return __builtin_bit_cast(u32, v); }
__device__ __forceinline__ float bflo(u32 w) { return __uint_as_float(w << 16); }
__device__ __forceinline__ float bfhi(u32 w) { return __uint_as_float(w & 0xffff0000u); }
__device__ __forceinline__ u32 nosent(u32 v) { return v == SENT ? 0x7FC07FC0u : v; }
__device__ __forceinline__ v4u ldnt(const void* p) { return __builtin_nontemporal_load((const GAS v4u*)p); }
__device__ __forceinline__ v4f mfma(v4u a, v4u b, v4f c) {
  return __builtin_amdgcn_mfma_f32_16x16x32_bf16(__builtin_bit_cast(bf16x8, a), __builtin_bit_cast(bf16x8, b), c, 0, 0, 0);
}
__device__ __forceinline__ v4f mfma16(v2u a, v2u b, v4f c) {
  return __builtin_amdgcn_mfma_f32_16x16x16bf16_1k(__builtin_bit_cast(s16x4, a), __builtin_bit_cast(s16x4, b), c, 0, 0, 0);
}
template <int CTRL> __device__ __forceinline__ float dpp(float x) {
  return __builtin_bit_cast(float, __builtin_amdgcn_mov_dpp(__builtin_bit_cast(int, x), CTRL, 0xf, 0xf, true));
}
template <int CTRL, int RM> __device__ __forceinline__ float dppm(float x) {
  return __builtin_bit_cast(float, __builtin_amdgcn_update_dpp(0, __builtin_bit_cast(int, x), CTRL, RM, 0xf, false));
}
constexpr int XOR1 = 0xB1, XOR2 = 0x4E, XOR7 = 0x141, XOR15 = 0x140, XOR8 = 0x128;
__device__ __forceinline__ float x16_max(float x) {
  auto s = __builtin_amdgcn_permlane16_swap(__float_as_uint(x), __float_as_uint(x), false, false);
  x = fmaxf(__uint_as_float(s[0]), __uint_as_float(s[1]));
  auto t = __builtin_amdgcn_permlane32_swap(__float_as_uint(x), __float_as_uint(x), false, false);
  return fmaxf(__uint_as_float(t[0]), __uint_as_float(t[1]));
}
__device__ __forceinline__ float wave_sum63(float x) {
  x += dpp<XOR1>(x); x += dpp<XOR2>(x); x += dpp<XOR7>(x); x += dpp<XOR15>(x);
  x += dppm<0x142, 0xa>(x);
  x += dppm<0x143, 0xc>(x);
  return x;
}
__device__ __forceinline__ float max8(const float (&s)[8]) {
  return fmaxf(fmaxf(fmaxf(s[0], s[1]), fmaxf(s[2], s[3])), fmaxf(fmaxf(s[4], s[5]), fmaxf(s[6], s[7])));
}
__device__ __forceinline__ u32 umax4(v4u v) { return max(max(v.x, v.y), max(v.z, v.w)); }
__device__ __forceinline__ u32 umin4(v4u v) { return min(min(v.x, v.y), min(v.z, v.w)); }
#define BAR() do { __builtin_amdgcn_sched_barrier(0); __syncthreads(); __builtin_amdgcn_sched_barrier(0); } while (0)
#define LGKM0() asm volatile("s_waitcnt lgkmcnt(0)" ::: "memory")
#define VM0() asm volatile("s_waitcnt vmcnt(0)" ::: "memory")
#define SB() __builtin_amdgcn_sched_barrier(0)
#define POLL _Pragma("nounroll") for (u32 spins = 0; spins < SPIN_CAP; ++spins)
#define KEEP8(a) asm volatile("" : "+v"(a[0]), "+v"(a[1]), "+v"(a[2]), "+v"(a[3]), "+v"(a[4]), "+v"(a[5]), "+v"(a[6]), "+v"(a[7]))
__device__ __forceinline__ u64 clk() { return __builtin_amdgcn_s_memtime(); }
__device__ __forceinline__ void wait_until(u64 t) { while (clk() < t) __builtin_amdgcn_s_sleep(1); }

#define STAMP(id) do { } while (0)
#define STAMP_INIT() do { } while (0)
#define STAMP_DUMP() do { } while (0)

__device__ __forceinline__ void tr8i(v4u* r) {
  v4u f[8];
#pragma unroll
  for (int c = 0; c < 8; ++c) {
    const u32 sel = (c & 1) ? 0x07060302u : 0x05040100u;
#pragma unroll
    for (int m = 0; m < 4; ++m) f[c][m] = __builtin_amdgcn_perm(r[2 * m + 1][c >> 1], r[2 * m][c >> 1], sel);
  }
#pragma unroll
  for (int c = 0; c < 8; ++c) r[c] = f[c];
}
__device__ __forceinline__ v2u tr4c(const v4u* r, int c) {
  const u32 sel = (c & 1) ? 0x07060302u : 0x05040100u;
  return v2u{__builtin_amdgcn_perm(r[1][c >> 1], r[0][c >> 1], sel), __builtin_amdgcn_perm(r[3][c >> 1], r[2][c >> 1], sel)};
}

template <bool AW>
__device__ __forceinline__ void path(const Params& p, LAS unsigned char* L, const int v, const u32 cpf) {
  const int tid = threadIdx.x, lane = tid & 63, w = v & 3;
  const int cu = blockIdx.x, g = cu & 7, j = cu >> 3, role = j >> 3, ri = j & 7;
  const int q4 = lane >> 4, n16 = lane & 15, kr = n16 >> 3, cb = n16 & 7;
  const __amdgpu_buffer_rsrc_t rs = __builtin_amdgcn_make_buffer_rsrc(p.ws, 0, WS_BYTES, 0x00020000);
  constexpr float LOG2E = 1.4426950408889634f;
  const v4f z4 = {0.f, 0.f, 0.f, 0.f};
  const int nblk = g < 4 ? 4 : 3;
  const int rng = (n16 >> 2) & 1;
  volatile LAS u32* flg = (volatile LAS u32*)(L + L_FLG);
  const LAS float* rd = (const LAS float*)(L + L_RED) + lane;
  const bool useful = q4 < 2 && q4 == kr;
  auto put_red = [&](v4f (&acc)[8], int slot) {
    KEEP8(acc);
    if (useful) {
      LAS float* dst = (LAS float*)(L + L_RED) + (slot * 2 + kr) * 64 + cb * 8;
#pragma unroll
      for (int c = 0; c < 8; ++c) dst[c] = acc[c][0];
    }
  };
  auto sum8 = [&](const LAS float* r) {
    float t[8];
#pragma unroll
    for (int k = 0; k < 8; ++k) t[k] = r[64 * k];
    return ((t[0] + t[1]) + (t[2] + t[3])) + ((t[4] + t[5]) + (t[6] + t[7]));
  };

  v4u ra[AW ? 16 : 1];
  v2u qw[AW ? 1 : 8];
  float xr;
  u32 tag;
  if constexpr (AW) {
    if (tid < 16) *(LAS u32*)(L + L_FLG + tid * 4) = 0u;
    STAMP_INIT();
    BAR();
    const u32 ep = __builtin_amdgcn_raw_buffer_load_b32(rs, WS_EPOCH, 0, AUX_SC1);
    const u32 xraw = *(const GAS u16*)(p.x + tid);
    const size_t kb = ((size_t)g * SK + (SK - WIN) + (w * 32 + j) * 16) * 128;
    const u64 t0 = clk();
    if (w == 3) {
      const v4u sv = {SENT, SENT, SENT, SENT};
      const int slot = WS_H2 + (g * 32 + j) * H2_SLOT;
      __builtin_amdgcn_raw_buffer_store_b128(sv, rs, slot + lane * 16, 0, AUX_SC1);
      __builtin_amdgcn_raw_buffer_store_b128(sv, rs, slot + H2_L + lane * 16, 0, AUX_SC1);
      __builtin_amdgcn_raw_buffer_store_b128(sv, rs, lane < 16 ? WS_H1B + g * 8192 + role * 2048 + ri * 256 + lane * 16 : lane < 18 ? slot + (lane - 16) * H2_L + 1024 : WS_BYTES, 0, AUX_SC1);
      int off = WS_BYTES;
      if (role < 2 && lane < 16) off = WS_H3 + (lane >> 3) * 16384 + g * 2048 + j * 128 + (lane & 7) * 16;
      if (role == 2) off = WS_H4 + (lane >> 5) * 32768 + g * 4096 + ri * 512 + (lane & 31) * 16;
      if (role == 3 && lane < 32) off = WS_H5 + (lane >> 4) * 16384 + g * 2048 + ri * 256 + (lane & 15) * 16;
      __builtin_amdgcn_raw_buffer_store_b128(sv, rs, off, 0, AUX_SC1);
      VM0();
      const u32 t = __builtin_amdgcn_readfirstlane(ep) + 1u;
      if (lane == 0) __builtin_amdgcn_raw_buffer_store_b64(v2u{t, t}, rs, WS_RDY + cu * 8, 0, AUX_SC1);
    }
    wait_until(t0 + D_KV);
#pragma unroll
    for (int i = 0; i < 16; ++i) {
      if (i == KVW1) { SB(); POLL { if (flg[5] != 0u) break; __builtin_amdgcn_s_sleep(1); } SB(); }
      if (i < 8) ra[i] = ldnt(p.kc + kb + ((i >> 2) * 2048 + n16) * 128 + (i & 3) * 32 + q4 * 8);
      else { const int e = i - 8; ra[i] = ldnt(p.vc + kb + (2048 * (e >> 2) + 4 * q4 + (e & 3)) * 128 + n16 * 8); }
    }
    SB();
    if (w == 0) STAMP(15);
    tag = __builtin_amdgcn_readfirstlane(ep) + 1u;
    xr = bflo(xraw);
    asm volatile("" :: "v"(cpf));
  } else {
    v4u wqr[4];
    const v4u xm = *(const GAS v4u*)(p.x + lane * 8);
    const v2u xa = *(const GAS v2u*)(p.x + 128 * role + 32 * w + 16 * rng + 4 * q4);
    const u32 ep = __builtin_amdgcn_raw_buffer_load_b32(rs, WS_EPOCH, 0, AUX_SC1);
    const u32 xraw = *(const GAS u16*)(p.x + tid);
    const u16* src = p.wq + (size_t)(128 * role + 32 * w + 16 * kr + 4 * q4) * 4096 + g * 512 + ri * 64 + cb * 8;
#pragma unroll
    for (int e = 0; e < 4; ++e) wqr[e] = ldnt(src + (size_t)e * 4096);
    BAR();
    float s1 = 0.f, s2 = 0.f;
#pragma unroll
    for (int k = 0; k < 4; ++k) {
      const float a0 = bflo(xm[k]), a1 = bfhi(xm[k]);
      s1 += a0 + a1; s2 = __builtin_fmaf(a0, a0, __builtin_fmaf(a1, a1, s2));
    }
    const float mean = __builtin_bit_cast(float, __builtin_amdgcn_readlane(__builtin_bit_cast(int, wave_sum63(s1)), 63)) * (1.f / 512.f);
    const float ex2 = __builtin_bit_cast(float, __builtin_amdgcn_readlane(__builtin_bit_cast(int, wave_sum63(s2)), 63)) * (1.f / 512.f);
    const float var = rbf(fmaxf(__builtin_fmaf(-mean, mean, ex2), 0.f));
    const float inv = __builtin_amdgcn_rcpf(rbf(__builtin_amdgcn_sqrtf(rbf(var + 1e-5f))));
    const float mb = rbf(mean);
    v2u a;
#pragma unroll
    for (int k = 0; k < 2; ++k) { const u32 d = pk2(bflo(xa[k]) - mb, bfhi(xa[k]) - mb); a[k] = pk2(bflo(d) * inv, bfhi(d) * inv); }
    if (w == 0) STAMP(1);
    v4f acc[8];
#pragma unroll
    for (int c = 0; c < 8; ++c) { qw[c] = tr4c(wqr, c); acc[c] = mfma16(a, qw[c], z4); }
    put_red(acc, w);
    LGKM0();
    tag = __builtin_amdgcn_readfirstlane(ep) + 1u;
    xr = bflo(xraw);
    flg[w] = 1u;
    if (w == 0) {
      POLL { const v4u f = *(volatile LAS v4u*)(L + L_FLG); if (umin4(f) != 0u) break; }
      __builtin_amdgcn_raw_buffer_store_b64(v2u{__float_as_uint(sum8(rd)), tag}, rs, WS_H1A + g * 16384 + role * 4096 + ri * 512 + lane * 8, 0, AUX_SC1);
      flg[4] = 1u;
      STAMP(2);
    } else { POLL { if (flg[4] != 0u) break; __builtin_amdgcn_s_sleep(1); } }
    const u64 tq = clk();
    if (w < 2) {
      wait_until(tq + D_H10);
      u32 qa, qb;
      POLL {
        v4u t[8];
#pragma unroll
        for (int k = 0; k < 8; ++k) t[k] = __builtin_amdgcn_raw_buffer_load_b128(rs, WS_H1A + g * 16384 + (k & 3) * 4096 + (2 * w + (k >> 2)) * 1024 + lane * 16, 0, AUX_SC1);
        SB(); if (w == 1) flg[5] = 1u; SB();
        u32 bad = 0;
#pragma unroll
        for (int k = 0; k < 8; ++k) bad |= (t[k].y ^ tag) | (t[k].w ^ tag);
        qa = pk2((__uint_as_float(t[0].x) + __uint_as_float(t[1].x)) + (__uint_as_float(t[2].x) + __uint_as_float(t[3].x)),
                 (__uint_as_float(t[0].z) + __uint_as_float(t[1].z)) + (__uint_as_float(t[2].z) + __uint_as_float(t[3].z)));
        qb = pk2((__uint_as_float(t[4].x) + __uint_as_float(t[5].x)) + (__uint_as_float(t[6].x) + __uint_as_float(t[7].x)),
                 (__uint_as_float(t[4].z) + __uint_as_float(t[5].z)) + (__uint_as_float(t[6].z) + __uint_as_float(t[7].z)));
        if (!__any(bad != 0u)) break;
        __builtin_amdgcn_s_sleep(4);
      }
      *(LAS u32*)(L + L_QS + w * 512 + lane * 4) = qa;
      *(LAS u32*)(L + L_QS + w * 512 + 256 + lane * 4) = qb;
      LGKM0();
      flg[8 + 2 * w] = 1u; flg[9 + 2 * w] = 1u;
      if (w == 0) STAMP(14);
      if (w == 1) {
        POLL {
          v4u t[2];
#pragma unroll
          for (int k = 0; k < 2; ++k) t[k] = __builtin_amdgcn_raw_buffer_load_b128(rs, WS_RDY + k * 1024 + lane * 16, 0, AUX_SC1);
          if (!__any((((t[0].x ^ tag) | (t[0].z ^ tag)) | ((t[1].x ^ tag) | (t[1].z ^ tag))) != 0u)) break;
          __builtin_amdgcn_s_sleep(4);
        }
        STAMP(30);
      }
    } else {
      const int hi = w & 1;
      if (role < 2) {
        wait_until(tq + D_W);
        const u16* wsrc = p.wo + (size_t)(g * 512 + 256 * hi + 16 * (n16 >> 2) + 4 * q4) * 512 + j * 32 + (n16 & 3) * 8;
#pragma unroll
        for (int i = 0; i < 16; ++i)
          __builtin_amdgcn_global_load_lds((const GAS u32*)(wsrc + ((i >> 2) * 64 + (i & 3)) * 512), (LAS u32*)(L + L_W + ((hi * 4 + (i >> 2)) * 8 + (i & 3)) * 1024), 16, 0, 2);
      } else {
        wait_until(tq + D_W2);
        const int rl = 32 * kr + 8 * q4;
        const u16* wsrc; int pitch = 512, tstep;
        if (role == 2) {
          const int bi = ri >> 1; const int blk = bi < nblk ? g + 8 * bi : g;
          pitch = FF; tstep = 64 * FF; wsrc = (hi ? p.wu : p.wg) + (size_t)(256 * (ri & 1) + rl) * FF + blk * 64 + cb * 8;
        } else { tstep = 8 * 64 * 512; wsrc = p.wd + (size_t)(g * 64 + rl) * 512 + ri * 64 + cb * 8; }
        const int nt = role == 3 ? (hi ? nblk : 0) : 4;
        _Pragma("nounroll") for (int t = 0; t < nt; ++t) {
          asm volatile("s_waitcnt vmcnt(%0)" :: "i"(DMAW) : "memory");
#pragma unroll
          for (int e = 0; e < 8; ++e)
            __builtin_amdgcn_global_load_lds((const GAS u32*)(wsrc + (size_t)t * tstep + (size_t)e * pitch), (LAS u32*)(L + L_W + ((hi * 4 + t) * 8 + e) * 1024), 16, 0, 2);
        }
      }
      VM0();
      if (w == 3) STAMP(13);
    }
  }

  auto gather_x = [&](int base) {
    float s;
    POLL {
      u32 t[8];
#pragma unroll
      for (int k = 0; k < 8; ++k) t[k] = __builtin_amdgcn_raw_buffer_load_b32(rs, base + k * 2048 + tid * 4, 0, AUX_SC1);
      s = ((__uint_as_float(t[0]) + __uint_as_float(t[1])) + (__uint_as_float(t[2]) + __uint_as_float(t[3]))) +
          ((__uint_as_float(t[4]) + __uint_as_float(t[5])) + (__uint_as_float(t[6]) + __uint_as_float(t[7])));
      if (!__any(s != s)) break;
      const u32 mx = max(max(max(t[0], t[1]), max(t[2], t[3])), max(max(t[4], t[5]), max(t[6], t[7])));
      if (!__any(mx == SENT)) break;
      __builtin_amdgcn_s_sleep(2);
    }
    xr = rbf(xr + rbf(s));
  };
  auto layernorm = [&]() {
    const float s1 = wave_sum63(xr), s2 = wave_sum63(xr * xr);
    if (lane == 63) *(LAS v2f*)(L + L_ST + v * 8) = v2f{s1, s2};
    BAR();
    const v4f a = *(const LAS v4f*)(L + L_ST), b = *(const LAS v4f*)(L + L_ST + 16), c = *(const LAS v4f*)(L + L_ST + 32), d = *(const LAS v4f*)(L + L_ST + 48);
    const v4f t = (a + b) + (c + d);
    const float mean = (t[0] + t[2]) * (1.f / 512.f), ex2 = (t[1] + t[3]) * (1.f / 512.f);
    const float var = rbf(fmaxf(__builtin_fmaf(-mean, mean, ex2), 0.f));
    const float inv = __builtin_amdgcn_rcpf(rbf(__builtin_amdgcn_sqrtf(rbf(var + 1e-5f))));
    *(LAS __bf16*)(L + L_ACT + tid * 2) = (__bf16)(rbf(xr - rbf(mean)) * inv);
    BAR();
  };
  v4u wr[8];
  auto gemv_tile = [&](int blk, int slot) {
    const v4u a = *(const LAS v4u*)(L + L_ACT + (64 * blk + 32 * rng + 8 * q4) * 2);
    v4f acc[8];
#pragma unroll
    for (int c = 0; c < 8; ++c) acc[c] = mfma(a, wr[c], z4);
    put_red(acc, slot);
  };
  auto hint_put = [&](int idx) {
    if (ri == 0 && v == 4 && lane == 0) __builtin_amdgcn_raw_buffer_store_b32(tag, rs, WS_CAN + (idx * 8 + g) * 128, 0, AUX_SC1);
  };
  auto hint_wait = [&](int idx, int delta) {
    if (v == 0) {
      POLL {
        if (__builtin_amdgcn_readfirstlane(__builtin_amdgcn_raw_buffer_load_b32(rs, WS_CAN + (idx * 8 + g) * 128, 0, AUX_SC1)) == tag) break;
        __builtin_amdgcn_s_sleep(2);
      }
    }
    BAR();
    wait_until(clk() + delta);
  };

  _Pragma("nounroll") for (int l = 0; l < 2; ++l) {
    if constexpr (AW) {
      POLL { const v4u f = *(volatile LAS v4u*)(L + L_FLG + 32); if (umin4(f) > (u32)l) break; }
      if (w == 0) STAMP(l * 16 + 3);
      v4u qf[4];
#pragma unroll
      for (int s = 0; s < 4; ++s) qf[s] = *(volatile LAS v4u*)(L + L_QS + ((n16 >> 2) * 128 + s * 32 + q4 * 8) * 2);
      v4f sc[2];
#pragma unroll
      for (int t = 0; t < 2; ++t) {
        sc[t] = z4;
#pragma unroll
        for (int s = 0; s < 4; ++s) sc[t] = mfma(ra[t * 4 + s], qf[s], sc[t]);
      }
      float s8[8];
#pragma unroll
      for (int e = 0; e < 8; e += 2) {
        const u32 r1 = pk2(sc[e >> 2][e & 3], sc[e >> 2][(e & 3) + 1]);
        const u32 r2 = pk2(bflo(r1) * 0.08838834764831845f, bfhi(r1) * 0.08838834764831845f);
        s8[e] = bflo(r2); s8[e + 1] = bfhi(r2);
      }
      const float m = x16_max(max8(s8)) * LOG2E;
#pragma unroll
      for (int e = 0; e < 8; ++e) s8[e] = __builtin_amdgcn_exp2f(__builtin_fmaf(s8[e], LOG2E, -m));
      v4u pa = {pk2(s8[0], s8[1]), pk2(s8[2], s8[3]), pk2(s8[4], s8[5]), pk2(s8[6], s8[7])};
      if (w == 0) STAMP(l * 16 + 4);
      { v4u pk = pa; asm volatile("" : "+v"(pk)); pa = pk; }
      if (l == 0) { tr8i(ra + 8); STAMP(32 + w); }
      v4f o[8];
#pragma unroll
      for (int c = 0; c < 8; ++c) o[c] = mfma(pa, ra[8 + c], z4);
      const v4f ls = mfma(pa, v4u{0x3F803F80u, 0x3F803F80u, 0x3F803F80u, 0x3F803F80u}, z4);
      KEEP8(o);
      LAS float* dst = (LAS float*)(L + L_PO) + (w * 4 + q4) * 128 + n16 * 8;
#pragma unroll
      for (int c = 0; c < 8; ++c) dst[c] = o[c][0];
      if (n16 == 4 * q4) *(LAS v2f*)(L + L_PM + (q4 * 4 + w) * 8) = v2f{m, ls[0]};
      LGKM0();
      flg[12 + w] = l + 1;
      POLL { const v4u f = *(volatile LAS v4u*)(L + L_FLG + 48); if (umin4(f) > (u32)l) break; }
      if (w == 0) STAMP(l * 16 + 5);
      const v4f ml0 = *(volatile LAS v4f*)(L + L_PM + w * 32), ml1 = *(volatile LAS v4f*)(L + L_PM + w * 32 + 16);
      v2f pv[4];
#pragma unroll
      for (int u = 0; u < 4; ++u) pv[u] = *(volatile LAS v2f*)(L + L_PO + ((u * 4 + w) * 128 + lane * 2) * 4);
      const float M = fmaxf(fmaxf(ml0[0], ml0[2]), fmaxf(ml1[0], ml1[2]));
      const float s0 = __builtin_amdgcn_exp2f(ml0[0] - M), s1 = __builtin_amdgcn_exp2f(ml0[2] - M);
      const float s2 = __builtin_amdgcn_exp2f(ml1[0] - M), s3 = __builtin_amdgcn_exp2f(ml1[2] - M);
      const float Ls = (s0 * ml0[1] + s1 * ml0[3]) + (s2 * ml1[1] + s3 * ml1[3]);
      const float inv = __builtin_amdgcn_rcpf(Ls);
      const v2f ov = ((s0 * pv[0] + s1 * pv[1]) + (s2 * pv[2] + s3 * pv[3])) * inv;
      const int slot = WS_H2 + l * H2_L + (g * 32 + j) * H2_SLOT;
      __builtin_amdgcn_raw_buffer_store_b32(nosent(pk2(ov[0], ov[1])), rs, slot + w * 256 + lane * 4, 0, AUX_SC1);
      if (lane == 0) __builtin_amdgcn_raw_buffer_store_b32(nosent(__float_as_uint(M + __builtin_amdgcn_logf(Ls))), rs, slot + 1024 + w * 4, 0, AUX_SC1);
      if (w == 0) STAMP(l * 16 + 6);
    }
    BAR();
    const u64 tp = clk();
    if (l == 0) {
#pragma unroll
      for (int e = 0; e < 8; ++e) wr[e] = *(const LAS v4u*)(L + L_W + (v * 8 + e) * 1024 + lane * 16);
      tr8i(wr);
    }
    if (role < 2) {
      wait_until(tp + (l == 0 ? D_H20 : D_H2));
      v4u pr[4]; float u[4];
      const int hb = WS_H2 + l * H2_L + (g * 32 + 4 * q4) * H2_SLOT;
      const int po = hb + 16 * kr * H2_SLOT + v * 128 + cb * 16;
      const int lo = hb + 16 * (n16 & 1) * H2_SLOT + 1024 + (v >> 1) * 4;
      POLL {
#pragma unroll
        for (int e = 0; e < 4; ++e) u[e] = __uint_as_float(__builtin_amdgcn_raw_buffer_load_b32(rs, lo + e * H2_SLOT, 0, AUX_SC1));
#pragma unroll
        for (int e = 0; e < 4; ++e) pr[e] = __builtin_amdgcn_raw_buffer_load_b128(rs, po + e * H2_SLOT, 0, AUX_SC1);
        const u32 mx = max(max(max(umax4(pr[0]), umax4(pr[1])), max(umax4(pr[2]), umax4(pr[3]))),
                           max(max(__float_as_uint(u[0]), __float_as_uint(u[1])), max(__float_as_uint(u[2]), __float_as_uint(u[3]))));
        if (!__any(mx == SENT)) break;
        __builtin_amdgcn_s_sleep(2);
      }
      hint_put(l * 3 + 0);
      if (v == 4) STAMP(l * 16 + 7);
      float M = fmaxf(fmaxf(u[0], u[1]), fmaxf(u[2], u[3]));
      M = fmaxf(M, dpp<XOR1>(M));
      M = x16_max(M);
#pragma unroll
      for (int e = 0; e < 4; ++e) u[e] = __builtin_amdgcn_exp2f(u[e] - M);
      const v2u wa = {pk2(u[0], u[1]), pk2(u[2], u[3])};
      const v4f dn = mfma16(wa, v2u{0x3F803F80u, 0x3F803F80u}, z4);
      v4f d[8];
#pragma unroll
      for (int c = 0; c < 8; ++c) d[c] = mfma16(wa, tr4c(pr, c), z4);
      KEEP8(d);
      const float inv = __builtin_amdgcn_rcpf(dn[0] + dn[1]);
      float ov[8];
#pragma unroll
      for (int c = 0; c < 8; ++c) { const float t = kr ? d[c][1] : d[c][0]; ov[c] = (t + dpp<XOR8>(t)) * inv; }
      if (lane < 8) *(LAS v4u*)(L + L_ACT + (64 * v + 8 * cb) * 2) = v4u{pk2(ov[0], ov[1]), pk2(ov[2], ov[3]), pk2(ov[4], ov[5]), pk2(ov[6], ov[7])};
      LGKM0();
      {
        const v2u a = *(const LAS v2u*)(L + L_ACT + (64 * v + 16 * (n16 >> 2) + 4 * q4) * 2);
        v4f acc[8];
#pragma unroll
        for (int c = 0; c < 8; ++c) acc[c] = mfma16(a, v2u{wr[c].x, wr[c].y}, z4);
        KEEP8(acc);
        if (q4 == (n16 >> 2)) {
          LAS float* dst = (LAS float*)(L + L_RED) + (v * 4 + q4) * 32 + (n16 & 3) * 8;
#pragma unroll
          for (int c = 0; c < 8; ++c) dst[c] = acc[c][0];
        }
      }
      BAR();
      if (v == 4) {
        const float y = sum8(rd) + sum8(rd + 512);
        auto t = __builtin_amdgcn_permlane32_swap(__float_as_uint(y), __float_as_uint(y), false, false);
        const float z = __uint_as_float(t[0]) + __uint_as_float(t[1]);
        if (lane < 32) __builtin_amdgcn_raw_buffer_store_b32(nosent(__float_as_uint(z)), rs, WS_H3 + l * 16384 + g * 2048 + j * 128 + lane * 4, 0, AUX_SC1);
        STAMP(l * 16 + 8);
      }
    } else if (role == 2) {
      hint_wait(l * 3 + 0, D_H3);
      gather_x(WS_H3 + l * 16384);
      hint_put(l * 3 + 1);
      if (v == 4) STAMP(l * 16 + 9);
      layernorm();
      gemv_tile(4 * (ri & 1) + w, v);
      BAR();
      if (!AW && w < 2 && (ri >> 1) < nblk)
        __builtin_amdgcn_raw_buffer_store_b32(nosent(__float_as_uint(sum8(rd + w * 512))), rs, WS_H4 + l * 32768 + g * 4096 + ri * 512 + w * 256 + lane * 4, 0, AUX_SC1);
      if (v == 4) STAMP(l * 16 + 10);
    } else if (role == 3) {
      hint_wait(l * 3 + 1, 0);
      const u64 th = clk();
      if (l == 0) gather_x(WS_H3);
      if constexpr (!AW) {
        v4f acc[8];
        if (w < nblk) {
          wait_until(th + D_H4);
          float gg, uu;
          const int hb = WS_H4 + l * 32768 + g * 4096 + w * 1024 + lane * 4;
          POLL {
            u32 t[4];
#pragma unroll
            for (int k = 0; k < 4; ++k) t[k] = __builtin_amdgcn_raw_buffer_load_b32(rs, hb + k * 256, 0, AUX_SC1);
            gg = __uint_as_float(t[0]) + __uint_as_float(t[2]); uu = __uint_as_float(t[1]) + __uint_as_float(t[3]);
            const u32 mx = max(max(t[0], t[1]), max(t[2], t[3]));
            if (!__any(mx == SENT)) break;
            __builtin_amdgcn_s_sleep(2);
          }
          hint_put(l * 3 + 2);
          if (w == 0) STAMP(l * 16 + 11);
          const u32 gu = pk2(gg, uu);
          const float hv = rbf(__builtin_amdgcn_rcpf(1.f + __builtin_amdgcn_exp2f(-LOG2E * bflo(gu)))) * bfhi(gu);
          *(LAS __bf16*)(L + L_ACT + tid * 2) = (__bf16)hv;
          LGKM0();
          gemv_tile(v, w);
        } else {
#pragma unroll
          for (int c = 0; c < 8; ++c) acc[c] = z4;
          put_red(acc, w);
        }
      }
      BAR();
      if (v == 4) {
        __builtin_amdgcn_raw_buffer_store_b32(nosent(__float_as_uint(sum8(rd))), rs, WS_H5 + l * 16384 + g * 2048 + ri * 256 + lane * 4, 0, AUX_SC1);
        STAMP(l * 16 + 12);
      }
    }
    if (l == 1) break;
    if (role < 2) { hint_wait(1, 0); gather_x(WS_H3); }
    if (role != 3) hint_wait(2, D_H5); else wait_until(clk() + D_H5B);
    gather_x(WS_H5);
    if (v == 4) STAMP(17);
    layernorm();
    if constexpr (!AW) {
      const v2u a = *(const LAS v2u*)(L + L_ACT + (128 * role + 32 * w + 16 * rng + 4 * q4) * 2);
      v4f acc[8];
#pragma unroll
      for (int c = 0; c < 8; ++c) acc[c] = mfma16(a, qw[c], z4);
      put_red(acc, w);
    }
    BAR();
    if constexpr (!AW) {
      const u64 tq = clk();
      if (w == 0) {
        __builtin_amdgcn_raw_buffer_store_b32(nosent(__float_as_uint(sum8(rd))), rs, WS_H1B + g * 8192 + role * 2048 + ri * 256 + lane * 4, 0, AUX_SC1);
        STAMP(18);
      }
      wait_until(tq + D_H1);
      v2f qs;
      POLL {
        v2u t[4];
#pragma unroll
        for (int k = 0; k < 4; ++k) t[k] = __builtin_amdgcn_raw_buffer_load_b64(rs, WS_H1B + g * 8192 + k * 2048 + w * 512 + lane * 8, 0, AUX_SC1);
        qs = (__builtin_bit_cast(v2f, t[0]) + __builtin_bit_cast(v2f, t[1])) + (__builtin_bit_cast(v2f, t[2]) + __builtin_bit_cast(v2f, t[3]));
        const float c = qs[0] + qs[1];
        if (!__any(c != c)) break;
        const u32 mx = max(max(max(t[0].x, t[0].y), max(t[1].x, t[1].y)), max(max(t[2].x, t[2].y), max(t[3].x, t[3].y)));
        if (!__any(mx == SENT)) break;
        __builtin_amdgcn_s_sleep(2);
      }
      *(LAS u32*)(L + L_QS + w * 256 + lane * 4) = pk2(qs[0], qs[1]);
      LGKM0();
      flg[8 + w] = 2u;
    }
  }
  if (j == 0) {
    if (cu == 0 && v == 4 && lane == 0) __builtin_amdgcn_raw_buffer_store_b32(tag, rs, WS_EPOCH, 0, AUX_SC1);
    hint_wait(4, 0);
    if (v == g) gather_x(WS_H3 + 16384);
    hint_wait(5, D_H5);
    if (v == g) {
      gather_x(WS_H5 + 16384);
      *(GAS __bf16*)(p.out + tid) = (__bf16)xr;
      STAMP(29);
    }
  }
}

__global__ void __launch_bounds__(512, 2) fused_decode(Params p) {
  __shared__ __attribute__((aligned(16))) unsigned char lds_raw[LDS_BYTES];
  LAS unsigned char* L = (LAS unsigned char*)lds_raw;
  const int v = __builtin_amdgcn_readfirstlane(threadIdx.x >> 6);
  u32 cpf = 0;
  if (v == 0) {
    const u64 pc = __builtin_amdgcn_s_getpc() & ~255ull;
    const __amdgpu_buffer_rsrc_t rc = __builtin_amdgcn_make_buffer_rsrc((void*)pc, 0, CODE_BYTES, 0x00020000);
    const v4u t = __builtin_amdgcn_raw_buffer_load_b128(rc, (blockIdx.x >> 3) * 1024 + (threadIdx.x & 63) * 16, 0, 0);
    cpf = t.x;
  }
  asm volatile("" :: "s"(p.x), "s"(p.kc), "s"(p.vc), "s"(p.wq), "s"(p.wo), "s"(p.wg), "s"(p.wu), "s"(p.wd), "s"(p.out), "s"(p.ws));
  if (v < 4) path<true>(p, L, v, cpf);
  else path<false>(p, L, v, cpf);
  STAMP_DUMP();
}

extern "C" void kernel_launch(void* const* d_in, const int* in_sizes, int n_in, void* d_out, int out_size, void* d_ws, size_t ws_size,
                              hipStream_t stream) {
  Params p;
  p.x = (const u16*)d_in[0]; p.kc = (const u16*)d_in[1]; p.vc = (const u16*)d_in[2]; p.wq = (const u16*)d_in[3]; p.wo = (const u16*)d_in[4];
  p.wg = (const u16*)d_in[6]; p.wu = (const u16*)d_in[7]; p.wd = (const u16*)d_in[8]; p.out = (u16*)d_out; p.ws = (unsigned char*)d_ws;
  hipLaunchKernelGGL(fused_decode, dim3(256), dim3(512), 0, stream, p);
}
```

```cpp
#include <hip/hip_runtime.h>

typedef unsigned short u16;
typedef unsigned u32;
typedef unsigned long long u64;
typedef u32 v4u __attribute__((ext_vector_type(4)));
typedef u32 v2u __attribute__((ext_vector_type(2)));
typedef float v4f __attribute__((ext_vector_type(4)));
typedef float v2f __attribute__((ext_vector_type(2)));
typedef __bf16 bf16x8 __attribute__((ext_vector_type(8)));
typedef __bf16 bf16x2 __attribute__((ext_vector_type(2)));
typedef short s16x4 __attribute__((ext_vector_type(4)));
#define GAS __attribute__((address_space(1)))
#define LAS __attribute__((address_space(3)))

constexpr int SK = 32768, WIN = 4096, FF = 1792;
constexpr u32 SENT = 0xFFFFFFFFu;
constexpr int AUX_SC1 = 16;
constexpr u32 SPIN_CAP = 200000;

constexpr int WS_EPOCH = 0;
constexpr int WS_RDY = 4096;
constexpr int WS_CAN = 8192;
constexpr int WS_H1A = 16384;
constexpr int WS_H1B = WS_H1A + 131072;
constexpr int H2_SLOT = 1152, H2_L = 256 * H2_SLOT;
constexpr int WS_H2 = WS_H1B + 65536;
constexpr int WS_H3 = WS_H2 + 2 * H2_L;
constexpr int WS_H4 = WS_H3 + 32768;
constexpr int WS_H5 = WS_H4 + 65536;
constexpr int WS_END = WS_H5 + 32768;
constexpr int WS_STAMP = 2 << 20;
constexpr int WS_BYTES = 1 << 20;
static_assert(WS_END <= WS_BYTES, "ws map");

constexpr int L_QS = 0;
constexpr int L_PO = 1024;
constexpr int L_PM = 9216;
constexpr int L_ACT = 9472;
constexpr int L_ST = 10496;
constexpr int L_FLG = 10624;
constexpr int L_DEN = 10688;
constexpr int L_RED = 10752;
constexpr int L_W = 20480;
constexpr int LDS_BYTES = L_W + 65536;

#define CODE_BYTES 20992
#define KVREL 5
#define DMAW 16
#define KVW1 4
#define D_KV 0
#define D_H10 1400
#define D_H1 700
#define D_H2 700
#define D_H3 1200
#define D_H4 800
#define D_H5 600
#define D_H5B 700
#define D_H20 1200
#define D_W 2500
#define D_W2 8000

struct Params {
  const u16* x; const u16* kc; const u16* vc; const u16* wq; const u16* wo;
  const u16* wg; const u16* wu; const u16* wd; u16* out; unsigned char* ws;
};

__device__ __forceinline__ float rbf(float x) { return (float)(__bf16)x; }
__device__ __forceinline__ u32 pk2(float lo, float hi) { bf16x2 v = {(__bf16)lo, (__bf16)hi}; return __builtin_bit_cast(u32, v); }
__device__ __forceinline__ u32 pk2o(float lo, float hi) { u32 r = pk2(lo, hi); asm("" : "+v"(r)); return r; }
__device__ __forceinline__ float fmx(float a, float b) { return __builtin_amdgcn_fmed3f(a, b, __builtin_inff()); }
__device__ __forceinline__ float bflo(u32 w) { return __uint_as_float(w << 16); }
__device__ __forceinline__ float bfhi(u32 w) { return __uint_as_float(w & 0xffff0000u); }
__device__ __forceinline__ u32 nosent(u32 v) { return v == SENT ? 0x7FC07FC0u : v; }
__device__ __forceinline__ v4u ldnt(const void* p) { return __builtin_nontemporal_load((const GAS v4u*)p); }
__device__ __forceinline__ v4f mfma(v4u a, v4u b, v4f c) {
  return __builtin_amdgcn_mfma_f32_16x16x32_bf16(__builtin_bit_cast(bf16x8, a), __builtin_bit_cast(bf16x8, b), c, 0, 0, 0);
}
__device__ __forceinline__ v4f mfma16(v2u a, v2u b, v4f c) {
  return __builtin_amdgcn_mfma_f32_16x16x16bf16_1k(__builtin_bit_cast(s16x4, a), __builtin_bit_cast(s16x4, b), c, 0, 0, 0);
}
template <int CTRL> __device__ __forceinline__ float dpp(float x) {
  return __builtin_bit_cast(float, __builtin_amdgcn_mov_dpp(__builtin_bit_cast(int, x), CTRL, 0xf, 0xf, true));
}
template <int CTRL, int RM> __device__ __forceinline__ float dppm(float x) {
  return __builtin_bit_cast(float, __builtin_amdgcn_update_dpp(0, __builtin_bit_cast(int, x), CTRL, RM, 0xf, false));
}
constexpr int XOR1 = 0xB1, XOR2 = 0x4E, XOR7 = 0x141, XOR15 = 0x140, XOR8 = 0x128, ROR4 = 0x124;
__device__ __forceinline__ float x16_max(float x) {
  auto s = __builtin_amdgcn_permlane16_swap(__float_as_uint(x), __float_as_uint(x), false, false);
  x = fmx(__uint_as_float(s[0]), __uint_as_float(s[1]));
  auto t = __builtin_amdgcn_permlane32_swap(__float_as_uint(x), __float_as_uint(x), false, false);
  return fmx(__uint_as_float(t[0]), __uint_as_float(t[1]));
}
__device__ __forceinline__ float x16_sum(float x) {
  auto s = __builtin_amdgcn_permlane16_swap(__float_as_uint(x), __float_as_uint(x), false, false);
  x = __uint_as_float(s[0]) + __uint_as_float(s[1]);
  auto t = __builtin_amdgcn_permlane32_swap(__float_as_uint(x), __float_as_uint(x), false, false);
  return __uint_as_float(t[0]) + __uint_as_float(t[1]);
}
__device__ __forceinline__ void wave_sum63x2(float& a, float& b) {
  asm("s_nop 1\n\t"
      "v_add_f32_dpp %0, %0, %0 quad_perm:[1,0,3,2] row_mask:0xf bank_mask:0xf\n\t"
      "v_add_f32_dpp %1, %1, %1 quad_perm:[1,0,3,2] row_mask:0xf bank_mask:0xf\n\t"
      "s_nop 0\n\t"
      "v_add_f32_dpp %0, %0, %0 quad_perm:[2,3,0,1] row_mask:0xf bank_mask:0xf\n\t"
      "v_add_f32_dpp %1, %1, %1 quad_perm:[2,3,0,1] row_mask:0xf bank_mask:0xf\n\t"
      "s_nop 0\n\t"
      "v_add_f32_dpp %0, %0, %0 row_half_mirror row_mask:0xf bank_mask:0xf\n\t"
      "v_add_f32_dpp %1, %1, %1 row_half_mirror row_mask:0xf bank_mask:0xf\n\t"
      "s_nop 0\n\t"
      "v_add_f32_dpp %0, %0, %0 row_mirror row_mask:0xf bank_mask:0xf\n\t"
      "v_add_f32_dpp %1, %1, %1 row_mirror row_mask:0xf bank_mask:0xf\n\t"
      "s_nop 0\n\t"
      "v_add_f32_dpp %0, %0, %0 row_bcast:15 row_mask:0xa bank_mask:0xf\n\t"
      "v_add_f32_dpp %1, %1, %1 row_bcast:15 row_mask:0xa bank_mask:0xf\n\t"
      "s_nop 0\n\t"
      "v_add_f32_dpp %0, %0, %0 row_bcast:31 row_mask:0xc bank_mask:0xf\n\t"
      "v_add_f32_dpp %1, %1, %1 row_bcast:31 row_mask:0xc bank_mask:0xf\n\t"
      "s_nop 1"
      : "+v"(a), "+v"(b));
}
__device__ __forceinline__ float max8(const float (&s)[8]) {
  return fmx(fmx(fmx(s[0], s[1]), fmx(s[2], s[3])), fmx(fmx(s[4], s[5]), fmx(s[6], s[7])));
}
__device__ __forceinline__ u32 umax4(v4u v) { return max(max(v.x, v.y), max(v.z, v.w)); }
__device__ __forceinline__ u32 umin4(v4u v) { return min(min(v.x, v.y), min(v.z, v.w)); }
#define BAR() do { __builtin_amdgcn_sched_barrier(0); __syncthreads(); __builtin_amdgcn_sched_barrier(0); } while (0)
#define LGKM0() asm volatile("s_waitcnt lgkmcnt(0)" ::: "memory")
#define VM0() asm volatile("s_waitcnt vmcnt(0)" ::: "memory")
#define SB() __builtin_amdgcn_sched_barrier(0)
#define POLL _Pragma("nounroll") for (u32 spins = 0; spins < SPIN_CAP; ++spins)
#define KEEP8(a) asm volatile("" : "+v"(a[0]), "+v"(a[1]), "+v"(a[2]), "+v"(a[3]), "+v"(a[4]), "+v"(a[5]), "+v"(a[6]), "+v"(a[7]))
__device__ __forceinline__ u64 clk() { return __builtin_amdgcn_s_memtime(); }
__device__ __forceinline__ void wait_until(u64 t) { while (clk() < t) __builtin_amdgcn_s_sleep(1); }

#define STAMP(id) do { } while (0)
#define STAMP_INIT() do { } while (0)
#define STAMP_DUMP() do { } while (0)

__device__ __forceinline__ void tr8i(v4u* r) {
  v4u f[8];
#pragma unroll
  for (int c = 0; c < 8; ++c) {
    const u32 sel = (c & 1) ? 0x07060302u : 0x05040100u;
#pragma unroll
    for (int m = 0; m < 4; ++m) f[c][m] = __builtin_amdgcn_perm(r[2 * m + 1][c >> 1], r[2 * m][c >> 1], sel);
  }
#pragma unroll
  for (int c = 0; c < 8; ++c) r[c] = f[c];
}
__device__ __forceinline__ v2u tr4c(const v4u* r, int c) {
  const u32 sel = (c & 1) ? 0x07060302u : 0x05040100u;
  return v2u{__builtin_amdgcn_perm(r[1][c >> 1], r[0][c >> 1], sel), __builtin_amdgcn_perm(r[3][c >> 1], r[2][c >> 1], sel)};
}

template <bool AW>
__device__ __forceinline__ void path(const Params& p, LAS unsigned char* L, const int v, const u32 cpf) {
  const int tid = threadIdx.x, lane = tid & 63, w = v & 3;
  const int cu = blockIdx.x, g = cu & 7, j = cu >> 3, role = j >> 3, ri = j & 7;
  const int q4 = lane >> 4, n16 = lane & 15, kr = n16 >> 3, cb = n16 & 7;
  const __amdgpu_buffer_rsrc_t rs = __builtin_amdgcn_make_buffer_rsrc(p.ws, 0, WS_BYTES, 0x00020000);
  constexpr float LOG2E = 1.4426950408889634f;
  const v4f z4 = {0.f, 0.f, 0.f, 0.f};
  const int nblk = g < 4 ? 4 : 3;
  const int rng = (n16 >> 2) & 1;
  volatile LAS u32* flg = (volatile LAS u32*)(L + L_FLG);
  const LAS float* rd = (const LAS float*)(L + L_RED) + lane;
  const bool useful = q4 < 2 && q4 == kr;
  auto put_red = [&](v4f (&acc)[8], int slot) {
    KEEP8(acc);
    if (useful) {
      LAS float* dst = (LAS float*)(L + L_RED) + (slot * 2 + kr) * 64 + cb * 8;
#pragma unroll
      for (int c = 0; c < 8; ++c) dst[c] = acc[c][0];
    }
  };
  auto sum8 = [&](const LAS float* r) {
    float t[8];
#pragma unroll
    for (int k = 0; k < 8; ++k) t[k] = r[64 * k];
    return ((t[0] + t[1]) + (t[2] + t[3])) + ((t[4] + t[5]) + (t[6] + t[7]));
  };

  v4u ra[AW ? 16 : 1];
  v2u qw[AW ? 1 : 8];
  float xr;
  u32 tag;
  if constexpr (AW) {
    if (tid < 32) *(LAS u32*)(L + L_FLG + tid * 4) = 0u;
    STAMP_INIT();
    BAR();
    const u32 ep = __builtin_amdgcn_raw_buffer_load_b32(rs, WS_EPOCH, 0, AUX_SC1);
    const u32 xraw = *(const GAS u16*)(p.x + tid);
    const size_t kb = ((size_t)g * SK + (SK - WIN) + (w * 32 + j) * 16) * 128;
    const u64 t0 = clk();
    if (w == 3) {
      const v4u sv = {SENT, SENT, SENT, SENT};
      const int slot = WS_H2 + (g * 32 + j) * H2_SLOT;
      __builtin_amdgcn_raw_buffer_store_b128(sv, rs, slot + lane * 16, 0, AUX_SC1);
      __builtin_amdgcn_raw_buffer_store_b128(sv, rs, slot + H2_L + lane * 16, 0, AUX_SC1);
      __builtin_amdgcn_raw_buffer_store_b128(sv, rs, lane < 16 ? WS_H1B + g * 8192 + role * 2048 + ri * 256 + lane * 16 : lane < 20 ? slot + ((lane - 16) >> 1) * H2_L + 1024 + (lane & 1) * 16 : WS_BYTES, 0, AUX_SC1);
      int off = WS_BYTES;
      if (role < 2 && lane < 16) off = WS_H3 + (lane >> 3) * 16384 + g * 2048 + j * 128 + (lane & 7) * 16;
      if (role == 2) off = WS_H4 + (lane >> 5) * 32768 + g * 4096 + ri * 512 + (lane & 31) * 16;
      if (role == 3 && lane < 32) off = WS_H5 + (lane >> 4) * 16384 + g * 2048 + ri * 256 + (lane & 15) * 16;
      __builtin_amdgcn_raw_buffer_store_b128(sv, rs, off, 0, AUX_SC1);
      VM0();
      const u32 t = __builtin_amdgcn_readfirstlane(ep) + 1u;
      if (lane == 0) __builtin_amdgcn_raw_buffer_store_b64(v2u{t, t}, rs, WS_RDY + cu * 8, 0, AUX_SC1);
    }
    wait_until(t0 + D_KV);
#pragma unroll
    for (int i = 0; i < 16; ++i) {
      if (i == KVW1) { SB(); POLL { if (flg[5] != 0u) break; __builtin_amdgcn_s_sleep(1); } SB(); }
      if (i < 8) ra[i] = ldnt(p.kc + kb + ((i >> 2) * 2048 + n16) * 128 + (i & 3) * 32 + q4 * 8);
      else { const int e = i - 8; ra[i] = ldnt(p.vc + kb + (2048 * (e >> 2) + 4 * q4 + (e & 3)) * 128 + n16 * 8); }
    }
    SB();
    if (w == 0) STAMP(15);
    tag = __builtin_amdgcn_readfirstlane(ep) + 1u;
    xr = bflo(xraw);
    asm volatile("" :: "v"(cpf));
  } else {
    v4u wqr[4];
    const v4u xm = *(const GAS v4u*)(p.x + lane * 8);
    const v2u xa = *(const GAS v2u*)(p.x + 128 * role + 32 * w + 16 * rng + 4 * q4);
    const u32 ep = __builtin_amdgcn_raw_buffer_load_b32(rs, WS_EPOCH, 0, AUX_SC1);
    const u32 xraw = *(const GAS u16*)(p.x + tid);
    const u16* src = p.wq + (size_t)(128 * role + 32 * w + 16 * kr + 4 * q4) * 4096 + g * 512 + ri * 64 + cb * 8;
#pragma unroll
    for (int e = 0; e < 4; ++e) wqr[e] = ldnt(src + (size_t)e * 4096);
    BAR();
    float s1 = 0.f, s2 = 0.f;
#pragma unroll
    for (int k = 0; k < 4; ++k) {
      const float a0 = bflo(xm[k]), a1 = bfhi(xm[k]);
      s1 += a0 + a1; s2 = __builtin_fmaf(a0, a0, __builtin_fmaf(a1, a1, s2));
    }
    wave_sum63x2(s1, s2);
    const float mean = __builtin_bit_cast(float, __builtin_amdgcn_readlane(__builtin_bit_cast(int, s1), 63)) * (1.f / 512.f);
    const float ex2 = __builtin_bit_cast(float, __builtin_amdgcn_readlane(__builtin_bit_cast(int, s2), 63)) * (1.f / 512.f);
    const float var = rbf(fmaxf(__builtin_fmaf(-mean, mean, ex2), 0.f));
    const float inv = __builtin_amdgcn_rcpf(rbf(__builtin_amdgcn_sqrtf(rbf(var + 1e-5f))));
    const float mb = rbf(mean);
    v2u a;
#pragma unroll
    for (int k = 0; k < 2; ++k) { const u32 d = pk2(bflo(xa[k]) - mb, bfhi(xa[k]) - mb); a[k] = pk2(bflo(d) * inv, bfhi(d) * inv); }
    if (w == 0) STAMP(1);
    v4f acc[8];
#pragma unroll
    for (int c = 0; c < 8; ++c) { qw[c] = tr4c(wqr, c); acc[c] = mfma16(a, qw[c], z4); }
    put_red(acc, w);
    LGKM0();
    tag = __builtin_amdgcn_readfirstlane(ep) + 1u;
    xr = bflo(xraw);
    flg[w] = 1u;
    if (w == 0) {
      POLL { const v4u f = *(volatile LAS v4u*)(L + L_FLG); if (umin4(f) != 0u) break; }
      __builtin_amdgcn_raw_buffer_store_b64(v2u{__float_as_uint(sum8(rd)), tag}, rs, WS_H1A + g * 16384 + role * 4096 + ri * 512 + lane * 8, 0, AUX_SC1);
      flg[4] = 1u;
      STAMP(2);
    } else { POLL { if (flg[4] != 0u) break; __builtin_amdgcn_s_sleep(1); } }
    const u64 tq = clk();
    if (w < 2) {
      wait_until(tq + D_H10);
      u32 qa, qb;
      POLL {
        v4u t[8];
#pragma unroll
        for (int k = 0; k < 8; ++k) t[k] = __builtin_amdgcn_raw_buffer_load_b128(rs, WS_H1A + g * 16384 + (k & 3) * 4096 + (2 * w + (k >> 2)) * 1024 + lane * 16, 0, AUX_SC1);
        SB(); if (w == 1) flg[5] = 1u; SB();
        u32 bad = 0;
#pragma unroll
        for (int k = 0; k < 8; ++k) bad |= (t[k].y ^ tag) | (t[k].w ^ tag);
        qa = pk2((__uint_as_float(t[0].x) + __uint_as_float(t[1].x)) + (__uint_as_float(t[2].x) + __uint_as_float(t[3].x)),
                 (__uint_as_float(t[0].z) + __uint_as_float(t[1].z)) + (__uint_as_float(t[2].z) + __uint_as_float(t[3].z)));
        qb = pk2((__uint_as_float(t[4].x) + __uint_as_float(t[5].x)) + (__uint_as_float(t[6].x) + __uint_as_float(t[7].x)),
                 (__uint_as_float(t[4].z) + __uint_as_float(t[5].z)) + (__uint_as_float(t[6].z) + __uint_as_float(t[7].z)));
        if (!__any(bad != 0u)) break;
        __builtin_amdgcn_s_sleep(4);
      }
      *(LAS u32*)(L + L_QS + w * 512 + lane * 4) = qa;
      *(LAS u32*)(L + L_QS + w * 512 + 256 + lane * 4) = qb;
      LGKM0();
      flg[8 + 2 * w] = 1u; flg[9 + 2 * w] = 1u;
      if (w == 0) STAMP(14);
      if (w == 1) {
        POLL {
          v4u t[2];
#pragma unroll
          for (int k = 0; k < 2; ++k) t[k] = __builtin_amdgcn_raw_buffer_load_b128(rs, WS_RDY + k * 1024 + lane * 16, 0, AUX_SC1);
          if (!__any((((t[0].x ^ tag) | (t[0].z ^ tag)) | ((t[1].x ^ tag) | (t[1].z ^ tag))) != 0u)) break;
          __builtin_amdgcn_s_sleep(4);
        }
        STAMP(30);
      }
    } else {
      const int hi = w & 1;
      if (role < 2) {
        wait_until(tq + D_W);
        const u16* wsrc = p.wo + (size_t)(g * 512 + 256 * hi + 16 * (n16 >> 2) + 4 * q4) * 512 + j * 32 + (n16 & 3) * 8;
#pragma unroll
        for (int i = 0; i < 16; ++i)
          __builtin_amdgcn_global_load_lds((const GAS u32*)(wsrc + ((i >> 2) * 64 + (i & 3)) * 512), (LAS u32*)(L + L_W + ((hi * 4 + (i >> 2)) * 8 + (i & 3)) * 1024), 16, 0, 2);
      } else {
        wait_until(tq + D_W2);
        const int rl = 32 * kr + 8 * q4;
        const u16* wsrc; int pitch = 512, tstep;
        if (role == 2) {
          const int bi = ri >> 1; const int blk = bi < nblk ? g + 8 * bi : g;
          pitch = FF; tstep = 64 * FF; wsrc = (hi ? p.wu : p.wg) + (size_t)(256 * (ri & 1) + rl) * FF + blk * 64 + cb * 8;
        } else { tstep = 8 * 64 * 512; wsrc = p.wd + (size_t)(g * 64 + rl) * 512 + ri * 64 + cb * 8; }
        const int nt = role == 3 ? (hi ? nblk : 0) : 4;
        _Pragma("nounroll") for (int t = 0; t < nt; ++t) {
          asm volatile("s_waitcnt vmcnt(%0)" :: "i"(DMAW) : "memory");
#pragma unroll
          for (int e = 0; e < 8; ++e)
            __builtin_amdgcn_global_load_lds((const GAS u32*)(wsrc + (size_t)t * tstep + (size_t)e * pitch), (LAS u32*)(L + L_W + ((hi * 4 + t) * 8 + e) * 1024), 16, 0, 2);
        }
      }
      VM0();
      if (w == 3) STAMP(13);
    }
  }

  auto gather_x = [&](int base) {
    float s;
    POLL {
      u32 t[8];
#pragma unroll
      for (int k = 0; k < 8; ++k) t[k] = __builtin_amdgcn_raw_buffer_load_b32(rs, base + k * 2048 + tid * 4, 0, AUX_SC1);
      s = ((__uint_as_float(t[0]) + __uint_as_float(t[1])) + (__uint_as_float(t[2]) + __uint_as_float(t[3]))) +
          ((__uint_as_float(t[4]) + __uint_as_float(t[5])) + (__uint_as_float(t[6]) + __uint_as_float(t[7])));
      if (!__any(s != s)) break;
      const u32 mx = max(max(max(t[0], t[1]), max(t[2], t[3])), max(max(t[4], t[5]), max(t[6], t[7])));
      if (!__any(mx == SENT)) break;
      __builtin_amdgcn_s_sleep(2);
    }
    xr = rbf(xr + rbf(s));
  };
  auto layernorm = [&]() {
    float s1 = xr, s2 = xr * xr;
    wave_sum63x2(s1, s2);
    if (lane == 63) *(LAS v2f*)(L + L_ST + v * 8) = v2f{s1, s2};
    BAR();
    const v4f a = *(const LAS v4f*)(L + L_ST), b = *(const LAS v4f*)(L + L_ST + 16), c = *(const LAS v4f*)(L + L_ST + 32), d = *(const LAS v4f*)(L + L_ST + 48);
    const v4f t = (a + b) + (c + d);
    const float mean = (t[0] + t[2]) * (1.f / 512.f), ex2 = (t[1] + t[3]) * (1.f / 512.f);
    const float var = rbf(fmaxf(__builtin_fmaf(-mean, mean, ex2), 0.f));
    const float inv = __builtin_amdgcn_rcpf(rbf(__builtin_amdgcn_sqrtf(rbf(var + 1e-5f))));
    *(LAS __bf16*)(L + L_ACT + tid * 2) = (__bf16)(rbf(xr - rbf(mean)) * inv);
    BAR();
  };
  v4u wr[8];
  auto gemv_tile = [&](int blk, int slot) {
    const v4u a = *(const LAS v4u*)(L + L_ACT + (64 * blk + 32 * rng + 8 * q4) * 2);
    v4f acc[8];
#pragma unroll
    for (int c = 0; c < 8; ++c) acc[c] = mfma(a, wr[c], z4);
    put_red(acc, slot);
  };
  auto hint_put = [&](int idx) {
    if (ri == 0 && v == 4 && lane == 0) __builtin_amdgcn_raw_buffer_store_b32(tag, rs, WS_CAN + (idx * 8 + g) * 128, 0, AUX_SC1);
  };
  auto hint_wait = [&](int idx, int delta) {
    if (v == 0) {
      POLL {
        if (__builtin_amdgcn_readfirstlane(__builtin_amdgcn_raw_buffer_load_b32(rs, WS_CAN + (idx * 8 + g) * 128, 0, AUX_SC1)) == tag) break;
        __builtin_amdgcn_s_sleep(2);
      }
    }
    BAR();
    wait_until(clk() + delta);
  };

  _Pragma("nounroll") for (int l = 0; l < 2; ++l) {
    if constexpr (AW) {
      v4u qf[4];
      POLL {
        const v4u f = *(volatile LAS v4u*)(L + L_FLG + 32);
#pragma unroll
        for (int s = 0; s < 4; ++s) qf[s] = *(volatile LAS v4u*)(L + L_QS + ((n16 >> 2) * 128 + s * 32 + q4 * 8) * 2);
        if (umin4(f) > (u32)l) break;
      }
      if (w == 0) STAMP(l * 16 + 3);
      v4f sc[2];
#pragma unroll
      for (int t = 0; t < 2; ++t) {
        sc[t] = z4;
#pragma unroll
        for (int s = 0; s < 4; ++s) sc[t] = mfma(ra[t * 4 + s], qf[s], sc[t]);
      }
      float s8[8];
#pragma unroll
      for (int e = 0; e < 8; e += 2) {
        const u32 r1 = pk2o(sc[e >> 2][e & 3], sc[e >> 2][(e & 3) + 1]);
        const u32 r2 = pk2o(bflo(r1) * 0.08838834764831845f, bfhi(r1) * 0.08838834764831845f);
        s8[e] = bflo(r2) * LOG2E; s8[e + 1] = bfhi(r2) * LOG2E;
      }
      v4u pa = {pk2(__builtin_amdgcn_exp2f(s8[0]), __builtin_amdgcn_exp2f(s8[1])), pk2(__builtin_amdgcn_exp2f(s8[2]), __builtin_amdgcn_exp2f(s8[3])),
                pk2(__builtin_amdgcn_exp2f(s8[4]), __builtin_amdgcn_exp2f(s8[5])), pk2(__builtin_amdgcn_exp2f(s8[6]), __builtin_amdgcn_exp2f(s8[7]))};
      const v4u ones = {0x3F803F80u, 0x3F803F80u, 0x3F803F80u, 0x3F803F80u};
      v4f ls = mfma(pa, ones, z4);
      float m = 0.f;
      if (__any(!(ls[0] >= 0x1p-60f && ls[0] <= 0x1p40f))) {
        m = x16_max(max8(s8));
        pa = v4u{pk2(__builtin_amdgcn_exp2f(s8[0] - m), __builtin_amdgcn_exp2f(s8[1] - m)), pk2(__builtin_amdgcn_exp2f(s8[2] - m), __builtin_amdgcn_exp2f(s8[3] - m)),
                 pk2(__builtin_amdgcn_exp2f(s8[4] - m), __builtin_amdgcn_exp2f(s8[5] - m)), pk2(__builtin_amdgcn_exp2f(s8[6] - m), __builtin_amdgcn_exp2f(s8[7] - m))};
        ls = mfma(pa, ones, z4);
      }
      if (w == 0) STAMP(l * 16 + 4);
      if (l == 0) { tr8i(ra + 8); STAMP(32 + w); }
      v4f o[8];
#pragma unroll
      for (int c = 0; c < 8; ++c) o[c] = mfma(pa, ra[8 + c], z4);
      if (n16 == 4 * q4) *(LAS v2f*)(L + L_PM + (q4 * 4 + w) * 8) = v2f{ls[0], m};
      KEEP8(o);
      LAS float* dst = (LAS float*)(L + L_PO) + (w * 4 + q4) * 128 + n16 * 8;
#pragma unroll
      for (int c = 0; c < 8; ++c) dst[c] = o[c][0];
      SB();
      flg[12 + w] = l + 1;
      v4f ml0, ml1; v2f pv[4];
      POLL {
        const v4u f = *(volatile LAS v4u*)(L + L_FLG + 48);
        ml0 = *(volatile LAS v4f*)(L + L_PM + w * 32); ml1 = *(volatile LAS v4f*)(L + L_PM + w * 32 + 16);
#pragma unroll
        for (int u = 0; u < 4; ++u) pv[u] = *(volatile LAS v2f*)(L + L_PO + ((u * 4 + w) * 128 + lane * 2) * 4);
        if (umin4(f) > (u32)l) break;
      }
      if (w == 0) STAMP(l * 16 + 5);
      float M = 0.f;
      if (__any(((__float_as_uint(ml0[1]) | __float_as_uint(ml0[3])) | (__float_as_uint(ml1[1]) | __float_as_uint(ml1[3]))) != 0u)) {
        M = fmx(fmx(ml0[1], ml0[3]), fmx(ml1[1], ml1[3]));
        const float s0 = __builtin_amdgcn_exp2f(ml0[1] - M), s1 = __builtin_amdgcn_exp2f(ml0[3] - M);
        const float s2 = __builtin_amdgcn_exp2f(ml1[1] - M), s3 = __builtin_amdgcn_exp2f(ml1[3] - M);
        ml0[0] *= s0; ml0[2] *= s1; ml1[0] *= s2; ml1[2] *= s3;
        pv[0] *= s0; pv[1] *= s1; pv[2] *= s2; pv[3] *= s3;
      }
      const v2f ov = (pv[0] + pv[1]) + (pv[2] + pv[3]);
      const int slot = WS_H2 + l * H2_L + (g * 32 + j) * H2_SLOT;
      __builtin_amdgcn_raw_buffer_store_b32(nosent(pk2(ov[0], ov[1])), rs, slot + w * 256 + lane * 4, 0, AUX_SC1);
      if (lane == 0) __builtin_amdgcn_raw_buffer_store_b64(v2u{nosent(__float_as_uint((ml0[0] + ml0[2]) + (ml1[0] + ml1[2]))), nosent(__float_as_uint(M))}, rs, slot + 1024 + w * 8, 0, AUX_SC1);
      if (w == 0) STAMP(l * 16 + 6);
    }
    BAR();
    const u64 tp = clk();
    if (l == 0) {
#pragma unroll
      for (int e = 0; e < 8; ++e) wr[e] = *(const LAS v4u*)(L + L_W + (v * 8 + e) * 1024 + lane * 16);
      tr8i(wr);
    }
    if (role < 2) {
      wait_until(tp + (l == 0 ? D_H20 : D_H2));
      v4u pr[4];
      const int hb = WS_H2 + l * H2_L + (g * 32 + 16 * kr + 4 * q4) * H2_SLOT;
      if (v == 7) {
        v4u t;
        POLL {
          t = __builtin_amdgcn_raw_buffer_load_b128(rs, WS_H2 + l * H2_L + (g * 32 + (lane >> 1)) * H2_SLOT + 1024 + (lane & 1) * 16, 0, AUX_SC1);
#pragma unroll
          for (int e = 0; e < 4; ++e) pr[e] = __builtin_amdgcn_raw_buffer_load_b128(rs, hb + e * H2_SLOT + v * 128 + cb * 16, 0, AUX_SC1);
          if (!__any(max(umax4(t), max(max(umax4(pr[0]), umax4(pr[1])), max(umax4(pr[2]), umax4(pr[3])))) == SENT)) break;
          __builtin_amdgcn_s_sleep(2);
        }
        float a = __uint_as_float(t.x), b = __uint_as_float(t.z);
        a += dpp<XOR2>(a); b += dpp<XOR2>(b);
        a += dpp<ROR4>(a); b += dpp<ROR4>(b);
        a += dpp<XOR8>(a); b += dpp<XOR8>(b);
        a = x16_sum(a); b = x16_sum(b);
        if (__any((t.y | t.w) != 0u)) { a = -1.f; b = -1.f; }
        if (lane < 2) *(LAS v2f*)(L + L_DEN + lane * 8) = v2f{a, b};
        SB();
        *(volatile LAS u32*)(L + L_DEN + 16) = l + 1;
      } else {
        POLL {
#pragma unroll
          for (int e = 0; e < 4; ++e) pr[e] = __builtin_amdgcn_raw_buffer_load_b128(rs, hb + e * H2_SLOT + v * 128 + cb * 16, 0, AUX_SC1);
          if (!__any(max(max(umax4(pr[0]), umax4(pr[1])), max(umax4(pr[2]), umax4(pr[3]))) == SENT)) break;
          __builtin_amdgcn_s_sleep(2);
        }
      }
      hint_put(l * 3 + 0);
      if (v == 4) STAMP(l * 16 + 7);
      const u32 hm = (n16 & 1) ? 0xFFFF0000u : 0x0000FFFFu;
      const int mi = cb >> 1;
      LAS v4f* omw = (LAS v4f*)(L + L_PO + ((v * 2 + kr) * 64 + 8 * cb + 4 * (q4 & 1)) * 4);
      volatile LAS v4f* om = (volatile LAS v4f*)(L + L_PO + (v * 128 + 16 * (n16 >> 2) + 4 * q4) * 4);
      {
        const v4u sel = {mi == 0 ? hm & 0x3F803F80u : 0u, mi == 1 ? hm & 0x3F803F80u : 0u, mi == 2 ? hm & 0x3F803F80u : 0u, mi == 3 ? hm & 0x3F803F80u : 0u};
        v4f d = z4;
#pragma unroll
        for (int e = 0; e < 4; ++e) d = mfma(sel, pr[e], d);
        if ((q4 >> 1) == kr) *omw = d;
      }
      SB();
      v4f o0, o1; float den;
      POLL {
        const u32 f = *(volatile LAS u32*)(L + L_DEN + 16);
        den = *(volatile LAS float*)(L + L_DEN + (v >> 1) * 4);
        o0 = om[0]; o1 = om[16];
        if (f > (u32)l) break;
      }
      if (__any(den < 0.f)) {
        v2u le[4];
#pragma unroll
        for (int e = 0; e < 4; ++e) le[e] = __builtin_amdgcn_raw_buffer_load_b64(rs, hb + e * H2_SLOT + 1024 + (v >> 1) * 8, 0, AUX_SC1);
        float M = fmx(fmx(__uint_as_float(le[0].y), __uint_as_float(le[1].y)), fmx(__uint_as_float(le[2].y), __uint_as_float(le[3].y)));
        M = x16_max(fmx(M, dpp<XOR8>(M)));
        den = 0.f;
        v4f d = z4;
#pragma unroll
        for (int e = 0; e < 4; ++e) {
          const float wv = __builtin_amdgcn_exp2f(__uint_as_float(le[e].y) - M);
          const u32 wd = pk2(wv, wv) & hm;
          den += bflo(pk2(wv, wv)) * __uint_as_float(le[e].x);
          d = mfma(v4u{mi == 0 ? wd : 0u, mi == 1 ? wd : 0u, mi == 2 ? wd : 0u, mi == 3 ? wd : 0u}, pr[e], d);
        }
        if ((q4 >> 1) == kr) *omw = d;
        den = x16_sum(den + dpp<XOR8>(den));
        LGKM0();
        o0 = om[0]; o1 = om[16];
      }
      {
        const v4f oa = (o0 + o1) * __builtin_amdgcn_rcpf(den);
        const v2u a = {pk2(oa[0], oa[1]), pk2(oa[2], oa[3])};
        v4f acc[8];
#pragma unroll
        for (int c = 0; c < 8; ++c) acc[c] = mfma16(a, v2u{wr[c].x, wr[c].y}, z4);
        KEEP8(acc);
        if (q4 == (n16 >> 2)) {
          LAS float* dst = (LAS float*)(L + L_RED) + (v * 4 + q4) * 32 + (n16 & 3) * 8;
#pragma unroll
          for (int c = 0; c < 8; ++c) dst[c] = acc[c][0];
        }
      }
      BAR();
      if (v == 4) {
        const float y = sum8(rd) + sum8(rd + 512);
        auto t = __builtin_amdgcn_permlane32_swap(__float_as_uint(y), __float_as_uint(y), false, false);
        const float z = __uint_as_float(t[0]) + __uint_as_float(t[1]);
        if (lane < 32) __builtin_amdgcn_raw_buffer_store_b32(nosent(__float_as_uint(z)), rs, WS_H3 + l * 16384 + g * 2048 + j * 128 + lane * 4, 0, AUX_SC1);
        STAMP(l * 16 + 8);
      }
    } else if (role == 2) {
      hint_wait(l * 3 + 0, D_H3);
      gather_x(WS_H3 + l * 16384);
      hint_put(l * 3 + 1);
      if (v == 4) STAMP(l * 16 + 9);
      layernorm();
      gemv_tile(4 * (ri & 1) + w, v);
      BAR();
      if (!AW && w < 2 && (ri >> 1) < nblk)
        __builtin_amdgcn_raw_buffer_store_b32(nosent(__float_as_uint(sum8(rd + w * 512))), rs, WS_H4 + l * 32768 + g * 4096 + ri * 512 + w * 256 + lane * 4, 0, AUX_SC1);
      if (v == 4) STAMP(l * 16 + 10);
    } else if (role == 3) {
      hint_wait(l * 3 + 1, 0);
      const u64 th = clk();
      if (l == 0) gather_x(WS_H3);
      if constexpr (!AW) {
        v4f acc[8];
        if (w < nblk) {
          wait_until(th + D_H4);
          float gg, uu;
          const int hb = WS_H4 + l * 32768 + g * 4096 + w * 1024 + lane * 4;
          POLL {
            u32 t[4];
#pragma unroll
            for (int k = 0; k < 4; ++k) t[k] = __builtin_amdgcn_raw_buffer_load_b32(rs, hb + k * 256, 0, AUX_SC1);
            gg = __uint_as_float(t[0]) + __uint_as_float(t[2]); uu = __uint_as_float(t[1]) + __uint_as_float(t[3]);
            const u32 mx = max(max(t[0], t[1]), max(t[2], t[3]));
            if (!__any(mx == SENT)) break;
            __builtin_amdgcn_s_sleep(2);
          }
          hint_put(l * 3 + 2);
          if (w == 0) STAMP(l * 16 + 11);
          const u32 gu = pk2(gg, uu);
          const float hv = rbf(__builtin_amdgcn_rcpf(1.f + __builtin_amdgcn_exp2f(-LOG2E * bflo(gu)))) * bfhi(gu);
          *(LAS __bf16*)(L + L_ACT + tid * 2) = (__bf16)hv;
          LGKM0();
          gemv_tile(v, w);
        } else {
#pragma unroll
          for (int c = 0; c < 8; ++c) acc[c] = z4;
          put_red(acc, w);
        }
      }
      BAR();
      if (v == 4) {
        __builtin_amdgcn_raw_buffer_store_b32(nosent(__float_as_uint(sum8(rd))), rs, WS_H5 + l * 16384 + g * 2048 + ri * 256 + lane * 4, 0, AUX_SC1);
        STAMP(l * 16 + 12);
      }
    }
    if (l == 1) break;
    if (role < 2) { hint_wait(1, 0); gather_x(WS_H3); }
    if (role != 3) hint_wait(2, D_H5); else wait_until(clk() + D_H5B);
    gather_x(WS_H5);
    if (v == 4) STAMP(17);
    layernorm();
    if constexpr (!AW) {
      const v2u a = *(const LAS v2u*)(L + L_ACT + (128 * role + 32 * w + 16 * rng + 4 * q4) * 2);
      v4f acc[8];
#pragma unroll
      for (int c = 0; c < 8; ++c) acc[c] = mfma16(a, qw[c], z4);
      put_red(acc, w);
    }
    BAR();
    if constexpr (!AW) {
      const u64 tq = clk();
      if (w == 0) {
        __builtin_amdgcn_raw_buffer_store_b32(nosent(__float_as_uint(sum8(rd))), rs, WS_H1B + g * 8192 + role * 2048 + ri * 256 + lane * 4, 0, AUX_SC1);
        STAMP(18);
      }
      wait_until(tq + D_H1);
      v2f qs;
      POLL {
        v2u t[4];
#pragma unroll
        for (int k = 0; k < 4; ++k) t[k] = __builtin_amdgcn_raw_buffer_load_b64(rs, WS_H1B + g * 8192 + k * 2048 + w * 512 + lane * 8, 0, AUX_SC1);
        qs = (__builtin_bit_cast(v2f, t[0]) + __builtin_bit_cast(v2f, t[1])) + (__builtin_bit_cast(v2f, t[2]) + __builtin_bit_cast(v2f, t[3]));
        const float c = qs[0] + qs[1];
        if (!__any(c != c)) break;
        const u32 mx = max(max(max(t[0].x, t[0].y), max(t[1].x, t[1].y)), max(max(t[2].x, t[2].y), max(t[3].x, t[3].y)));
        if (!__any(mx == SENT)) break;
        __builtin_amdgcn_s_sleep(2);
      }
      *(LAS u32*)(L + L_QS + w * 256 + lane * 4) = pk2(qs[0], qs[1]);
      SB();
      flg[8 + w] = 2u;
    }
  }
  if (j == 0) {
    if (cu == 0 && v == 4 && lane == 0) __builtin_amdgcn_raw_buffer_store_b32(tag, rs, WS_EPOCH, 0, AUX_SC1);
    hint_wait(4, 0);
    if (v == g) gather_x(WS_H3 + 16384);
    hint_wait(5, D_H5);
    if (v == g) {
      gather_x(WS_H5 + 16384);
      *(GAS __bf16*)(p.out + tid) = (__bf16)xr;
      STAMP(29);
    }
  }
}

__global__ void __launch_bounds__(512, 2) fused_decode(Params p) {
  __shared__ __attribute__((aligned(16))) unsigned char lds_raw[LDS_BYTES];
  LAS unsigned char* L = (LAS unsigned char*)lds_raw;
  const int v = __builtin_amdgcn_readfirstlane(threadIdx.x >> 6);
  u32 cpf = 0;
  if (v == 0) {
    const u64 pc = __builtin_amdgcn_s_getpc() & ~255ull;
    const __amdgpu_buffer_rsrc_t rc = __builtin_amdgcn_make_buffer_rsrc((void*)pc, 0, CODE_BYTES, 0x00020000);
    const v4u t = __builtin_amdgcn_raw_buffer_load_b128(rc, (blockIdx.x >> 3) * 1024 + (threadIdx.x & 63) * 16, 0, 0);
    cpf = t.x;
  }
  asm volatile("" :: "s"(p.x), "s"(p.kc), "s"(p.vc), "s"(p.wq), "s"(p.wo), "s"(p.wg), "s"(p.wu), "s"(p.wd), "s"(p.out), "s"(p.ws));
  if (v < 4) path<true>(p, L, v, cpf);
  else path<false>(p, L, v, cpf);
  STAMP_DUMP();
}

extern "C" void kernel_launch(void* const* d_in, const int* in_sizes, int n_in, void* d_out, int out_size, void* d_ws, size_t ws_size,
                              hipStream_t stream) {
  Params p;
  p.x = (const u16*)d_in[0]; p.kc = (const u16*)d_in[1]; p.vc = (const u16*)d_in[2]; p.wq = (const u16*)d_in[3]; p.wo = (const u16*)d_in[4];
  p.wg = (const u16*)d_in[6]; p.wu = (const u16*)d_in[7]; p.wd = (const u16*)d_in[8]; p.out = (u16*)d_out; p.ws = (unsigned char*)d_ws;
  hipLaunchKernelGGL(fused_decode, dim3(256), dim3(512), 0, stream, p);
}
```
